```python
import torch

def _softmax(x, axis=-1):
    return torch.softmax(x, dim=axis)
def _gelu(x):
    return torch.nn.functional.gelu(x, approximate="tanh")
def _silu(x):
    return torch.nn.functional.silu(x)
def _sigmoid(x):
    return torch.sigmoid(x)
def _layernorm(x, axis=-1, eps=1e-5):
    m = x.mean(dim=axis, keepdim=True)
    v = x.var(dim=axis, keepdim=True, unbiased=False)
    return (x - m) / torch.sqrt(v + eps)
def _scatter_add(tgt, idx, src, axis=0):
    return tgt.clone().index_add_(axis, idx, src)
def _segment_sum(src, seg, n):
    out = torch.zeros((n,) + src.shape[1:], dtype=src.dtype)
    return out.index_add_(0, seg, src)
def _pad(x, pw):
    flat = tuple(v for pair in reversed(pw) for v in pair)
    return torch.nn.functional.pad(x, flat)

kernel_name = 'standalone__kv_cache_quant_int4_int2_kv_with_per_channel_or_per_token_scales__prompt'

def setup_inputs():

    _w = (lambda a: (lambda t: t.to(torch.bfloat16) if t.is_floating_point() else t)(torch.as_tensor(a)))
    B = 1
    Sq = 2048
    Skv = 2048
    Hq = 32
    Hkv = 4
    Dh = 128
    bits = 3
    G = -1
    if G <= 0 or G >= Skv:
        G = max(1, Skv // 2)
    Skv = (Skv // G) * G
    ngk = Skv // G
    qmax = (1 << bits) - 1
    return {
        "q": _w((lambda *s: torch.randn(*s, dtype=torch.bfloat16) * 1.6)(B, Hq, Sq, Dh)),
        "qk": _w(torch.randint(0, qmax + 1, (B, Hkv, Skv, Dh)).to(torch.int32)),
        "qv": _w(torch.randint(0, qmax + 1, (B, Hkv, Skv, Dh)).to(torch.int32)),
        "k_scales": _w(torch.abs((lambda *s: torch.randn(*s, dtype=torch.bfloat16) * 1.6)(B, Hkv, ngk, Dh)) * 0.05 + 0.02),
        "k_zeros": _w((lambda *s: torch.randn(*s, dtype=torch.bfloat16) * 1.6)(B, Hkv, ngk, Dh) + float(qmax) / 2.0),
        "v_scales": _w(torch.abs((lambda *s: torch.randn(*s, dtype=torch.bfloat16) * 0.05)(B, Hkv, Skv, 1)) * 0.05 + 0.02),
        "v_zeros": _w((lambda *s: torch.randn(*s, dtype=torch.bfloat16) * 0.05)(B, Hkv, Skv, 1) + float(qmax) / 2.0),
    }

def reference(q, qk, qv, k_scales, k_zeros, v_scales, v_zeros):
    B, Hq, Sq, Dh = q.shape
    Hkv, Skv = qk.shape[1], qk.shape[2]
    ngk = k_scales.shape[2]
    G = Skv // ngk
    ks = (lambda x, n, axis: torch.repeat_interleave(x, n, dim=axis))(k_scales, G, axis=2)
    kz = (lambda x, n, axis: torch.repeat_interleave(x, n, dim=axis))(k_zeros, G, axis=2)
    k = ((lambda x, dt: x.to(dt))(qk, q.dtype) - kz) * ks
    v = ((lambda x, dt: x.to(dt))(qv, q.dtype) - v_zeros) * v_scales
    rep = Hq // Hkv
    if rep > 1:
        k = (lambda x, n, axis: torch.repeat_interleave(x, n, dim=axis))(k, rep, axis=1)
        v = (lambda x, n, axis: torch.repeat_interleave(x, n, dim=axis))(v, rep, axis=1)
    scale = float(Dh) ** -0.5
    logits = torch.einsum("bhqd,bhkd->bhqk", q, k) * scale
    p = _softmax(logits, axis=-1)
    o = torch.einsum("bhqk,bhkd->bhqd", p, v)
    return o
```

```cpp
#include <hip/hip_runtime.h>
#include <hip/hip_bf16.h>

typedef unsigned short u16;
typedef float f32x2 __attribute__((ext_vector_type(2)));
typedef __bf16 bf16x2 __attribute__((ext_vector_type(2)));

constexpr int SKV = 2048, DH = 128, GRP = 1024;
constexpr int LDS_BYTES = 141312;
constexpr float CSC = 0.12751743082f;

__device__ __forceinline__ float bf2f(u16 x) { return __uint_as_float((unsigned)x << 16); }
__device__ __forceinline__ unsigned pkbf(float a, float b) { f32x2 v = {a, b}; bf16x2 r = __builtin_convertvector(v, bf16x2); return __builtin_bit_cast(unsigned, r); }
__device__ __forceinline__ float rbf(float a) { return __uint_as_float(pkbf(a, 0.f) << 16); }

__device__ __forceinline__ void slow_rows(const u16* __restrict__ q, const int* __restrict__ qk, const int* __restrict__ qv, const u16* __restrict__ ks, const u16* __restrict__ kz,
                                       const u16* __restrict__ vs, const u16* __restrict__ vz, u16* __restrict__ out, int lane) {
  const int r = lane >> 3, c = (lane & 7) * 16;
  for (int pass = 0; pass < 8; ++pass) {
    const int row = pass * 8 + r;
    float qf[16], o[16];
#pragma unroll
    for (int e = 0; e < 16; ++e) { qf[e] = bf2f(q[row * DH + c + e]); o[e] = 0.f; }
    float m = -__builtin_inff(), l = 0.f;
    for (int g = 0; g < 2; ++g) {
      float sc[16], zz[16];
#pragma unroll
      for (int e = 0; e < 16; ++e) { sc[e] = bf2f(ks[g * DH + c + e]); zz[e] = bf2f(kz[g * DH + c + e]); }
      for (int j = g * GRP; j < (g + 1) * GRP; ++j) {
        float s = 0.f;
#pragma unroll
        for (int e = 0; e < 16; ++e) s = fmaf(qf[e], rbf(rbf(rbf((float)qk[j * DH + c + e]) - zz[e]) * sc[e]), s);
        s += __shfl_xor(s, 1); s += __shfl_xor(s, 2); s += __shfl_xor(s, 4);
        s *= CSC;
        const float mn = fmaxf(m, s);
        const float sub = (mn == -__builtin_inff()) ? 0.f : mn;
        const float al = __builtin_amdgcn_exp2f(m - sub), p = __builtin_amdgcn_exp2f(s - sub);
        const float z = bf2f(vz[j]), sv = bf2f(vs[j]);
        l = l * al + p; m = mn;
#pragma unroll
        for (int e = 0; e < 16; ++e) o[e] = fmaf(p, rbf(rbf(rbf((float)qv[j * DH + c + e]) - z) * sv), o[e] * al);
      }
    }
    const float il = 1.f / l;
#pragma unroll
    for (int e = 0; e < 16; e += 2) *(unsigned*)(out + row * DH + c + e) = pkbf(o[e] * il, o[e + 1] * il);
  }
}

__global__ __launch_bounds__(256, 1) void attn_kernel(const u16* __restrict__ q, const int* __restrict__ qk, const int* __restrict__ qv, const u16* __restrict__ ks, const u16* __restrict__ kz,
                                                      const u16* __restrict__ vs, const u16* __restrict__ vz, char* __restrict__ ws, u16* __restrict__ out) {
  const unsigned b = blockIdx.x;
  const unsigned wid = __builtin_amdgcn_readfirstlane(threadIdx.x >> 6);
  unsigned bad;
  asm volatile(
    "L_begin_%=:\n\t"
    "s_load_dwordx16 s[8:23], %1, 0x0\n\t"
    "s_load_dwordx2 s[24:25], %1, 0x40\n\t"
    "v_mbcnt_lo_u32_b32 v234, -1, 0\n\t"
    "v_mbcnt_hi_u32_b32 v234, -1, v234\n\t"
    "s_getpc_b64 s[48:49]\n\t"
    "L_pc_%=:\n\t"
    "s_and_b32 s49, s49, 0xffff\n\t"
    "s_mov_b32 s50, L_end_%=-L_pc_%=\n\t"
    "s_mov_b32 s51, 0x20000\n\t"
    "v_lshlrev_b32 v237, 7, v234\n\t"
    "s_lshl_b32 s60, %3, 13\n\t"
    "buffer_load_dword v230, v237, s[48:51], s60 offen\n\t"
    "s_bfe_u32 s29, %2, 0x20001\n\t"
    "s_and_b32 s60, %2, 1\n\t"
    "s_lshl_b32 s60, s60, 5\n\t"
    "s_lshr_b32 s28, %2, 3\n\t"
    "s_add_u32 s28, s28, s60\n\t"
    "s_lshl_b32 s30, s29, 6\n\t"
    "s_add_u32 s30, s30, s28\n\t"
    "v_lshrrev_b32 v235, 4, v234\n\t"
    "v_and_b32 v236, 15, v234\n\t"
    "v_lshlrev_b32 v241, 4, v234\n\t"
    "s_and_b32 s55, %3, 1\n\t"
    "s_cmp_lt_u32 %3, 2\n\t"
    "s_cbranch_scc1 L_sk1_%=\n\t"
    "v_and_b32 v239, 1, v235\n\t"
    "v_lshlrev_b32 v238, 5, v236\n\t"
    "v_lshl_add_u32 v238, v239, 12, v238\n\t"
    "v_lshrrev_b32 v237, 5, v234\n\t"
    "v_lshl_add_u32 v238, v237, 4, v238\n\t"
    "s_lshl_b32 s61, s30, 14\n\t"
    "s_lshl_b32 s60, s55, 13\n\t"
    "s_add_u32 s60, s60, s61\n\t"
    "v_add_u32 v238, s60, v238\n\t"
    "s_lshl_b32 s60, s55, 5\n\t"
    "s_lshl_b32 s65, s30, 6\n\t"
    "s_add_u32 s60, s60, s65\n\t"
    "v_lshl_add_u32 v239, v239, 4, s60\n\t"
    "v_and_b32 v237, 12, v236\n\t"
    "v_add_u32 v237, v237, v236\n\t"
    "v_lshlrev_b32 v237, 9, v237\n\t"
    "v_lshl_add_u32 v237, v235, 5, v237\n\t"
    "s_lshl_b32 s60, s55, 11\n\t"
    "s_add_u32 s60, s60, s61\n\t"
    "v_add_u32 v237, s60, v237\n\t"
    "s_waitcnt lgkmcnt(0)\n\t"
    "global_load_dwordx4 v[96:99], v239, s[20:21]\n\t"
    "global_load_dwordx4 v[100:103], v239, s[18:19]\n\t"
    "global_load_dwordx4 v[64:67], v238, s[12:13] offset:0 nt\n\t"
    "global_load_dwordx4 v[68:71], v238, s[12:13] offset:512 nt\n\t"
    "global_load_dwordx4 v[72:75], v238, s[12:13] offset:1024 nt\n\t"
    "global_load_dwordx4 v[76:79], v238, s[12:13] offset:1536 nt\n\t"
    "global_load_dwordx4 v[80:83], v238, s[12:13] offset:2048 nt\n\t"
    "global_load_dwordx4 v[84:87], v238, s[12:13] offset:2560 nt\n\t"
    "global_load_dwordx4 v[88:91], v238, s[12:13] offset:3072 nt\n\t"
    "global_load_dwordx4 v[92:95], v238, s[12:13] offset:3584 nt\n\t"
    "global_load_dwordx4 v[0:3], v237, s[10:11] offset:0 nt\n\t"
    "global_load_dwordx4 v[4:7], v237, s[10:11] offset:16 nt\n\t"
    "global_load_dwordx4 v[8:11], v237, s[10:11] offset:128 nt\n\t"
    "global_load_dwordx4 v[12:15], v237, s[10:11] offset:144 nt\n\t"
    "global_load_dwordx4 v[16:19], v237, s[10:11] offset:256 nt\n\t"
    "global_load_dwordx4 v[20:23], v237, s[10:11] offset:272 nt\n\t"
    "global_load_dwordx4 v[24:27], v237, s[10:11] offset:384 nt\n\t"
    "global_load_dwordx4 v[28:31], v237, s[10:11] offset:400 nt\n\t"
    "s_mul_i32 s60, s29, 1056768\n\t"
    "s_add_u32 s26, s22, s60\n\t"
    "s_addc_u32 s27, s23, 0\n\t"
    "v_mov_b32 v237, 6144\n\t"
    "global_load_dwordx2 v[224:225], v237, s[26:27] sc1\n\t"
    "L_sk1_%=:\n\t"
    "s_cmp_lt_u32 %3, 2\n\t"
    "s_cbranch_scc0 L_sk2_%=\n\t"
    "v_lshlrev_b32 v239, 2, v234\n\t"
    "s_lshl_b32 s60, s29, 9\n\t"
    "v_add_u32 v238, s60, v239\n\t"
    "s_lshl_b32 s61, s30, 16\n\t"
    "v_mov_b32 v237, s61\n\t"
    "s_waitcnt lgkmcnt(0)\n\t"
    "global_load_dword v232, v237, s[8:9]\n\t"
    "global_load_dword v231, v237, s[24:25]\n\t"
    "global_load_dword v226, v238, s[14:15]\n\t"
    "global_load_dword v227, v238, s[14:15] offset:256\n\t"
    "global_load_dword v228, v238, s[16:17]\n\t"
    "global_load_dword v229, v238, s[16:17] offset:256\n\t"
    "L_sk2_%=:\n\t"
    "s_lshl_b32 s61, s30, 16\n\t"
    "s_add_u32 s44, s8, s61\n\t"
    "s_addc_u32 s45, s9, 0\n\t"
    "s_mul_i32 s60, s29, 1056768\n\t"
    "s_add_u32 s60, s60, 8192\n\t"
    "s_add_u32 s40, s22, s60\n\t"
    "s_addc_u32 s41, s23, 0\n\t"
    "s_lshl_b32 s60, %3, 14\n\t"
    "s_add_u32 s60, s60, s61\n\t"
    "s_add_u32 s48, s24, s60\n\t"
    "s_addc_u32 s49, s25, 0\n\t"
    "s_and_b32 s45, s45, 0xffff\n\t"
    "s_mov_b32 s46, 65536\n\t"
    "s_mov_b32 s47, 0x20000\n\t"
    "s_and_b32 s41, s41, 0xffff\n\t"
    "s_mov_b32 s42, 1048576\n\t"
    "s_mov_b32 s43, 0x20000\n\t"
    "s_and_b32 s49, s49, 0xffff\n\t"
    "s_mov_b32 s50, 16384\n\t"
    "s_mov_b32 s51, 0x20000\n\t"
    "v_mov_b32 v237, 132608\n\t"
    "v_mov_b32 v252, 0\n\t"
    "v_mov_b32 v253, 0\n\t"
    "ds_write_b64 v237, v[252:253]\n\t"
    "ds_write_b64 v237, v[252:253] offset:16\n\t"
    "s_waitcnt lgkmcnt(0)\n\t"
    "s_barrier\n\t"
    "s_cmp_lt_u32 %3, 2\n\t"
    "s_cbranch_scc0 L_sk3_%=\n\t"
    "v_xor_b32 v48, v236, v235\n\t"
    "v_lshlrev_b32 v48, 4, v48\n\t"
    "v_lshl_add_u32 v48, v235, 8, v48\n\t"
    "v_xor_b32 v49, v236, v235\n\t"
    "v_xor_b32 v49, 4, v49\n\t"
    "v_lshlrev_b32 v49, 4, v49\n\t"
    "v_lshl_add_u32 v49, v235, 8, v49\n\t"
    "v_xor_b32 v50, v236, v235\n\t"
    "v_xor_b32 v50, 8, v50\n\t"
    "v_lshlrev_b32 v50, 4, v50\n\t"
    "v_lshl_add_u32 v50, v235, 8, v50\n\t"
    "v_xor_b32 v51, v236, v235\n\t"
    "v_xor_b32 v51, 12, v51\n\t"
    "v_lshlrev_b32 v51, 4, v51\n\t"
    "v_lshl_add_u32 v51, v235, 8, v51\n\t"
    "s_lshl_b32 s60, %3, 14\n\t"
    "s_add_u32 s65, s60, 32768\n\t"
    "s_add_u32 m0, s60, 98304\n\t"
    "s_nop 0\n\t"
    "buffer_load_dwordx4 v48, s[44:47], s65 offen offset:0 nt lds\n\t"
    "buffer_load_dwordx4 v49, s[44:47], s65 offen offset:1024 nt lds\n\t"
    "buffer_load_dwordx4 v50, s[44:47], s65 offen offset:2048 nt lds\n\t"
    "buffer_load_dwordx4 v51, s[44:47], s65 offen offset:3072 nt lds\n\t"
    "s_lshl_b32 s60, %3, 14\n\t"
    "s_add_u32 s65, s60, 36864\n\t"
    "s_add_u32 m0, s60, 102400\n\t"
    "s_nop 0\n\t"
    "buffer_load_dwordx4 v48, s[44:47], s65 offen offset:0 nt lds\n\t"
    "buffer_load_dwordx4 v49, s[44:47], s65 offen offset:1024 nt lds\n\t"
    "buffer_load_dwordx4 v50, s[44:47], s65 offen offset:2048 nt lds\n\t"
    "buffer_load_dwordx4 v51, s[44:47], s65 offen offset:3072 nt lds\n\t"
    "s_lshl_b32 s60, %3, 14\n\t"
    "s_add_u32 s65, s60, 0\n\t"
    "s_add_u32 m0, s60, 65536\n\t"
    "s_nop 0\n\t"
    "buffer_load_dwordx4 v48, s[44:47], s65 offen offset:0 nt lds\n\t"
    "buffer_load_dwordx4 v49, s[44:47], s65 offen offset:1024 nt lds\n\t"
    "buffer_load_dwordx4 v50, s[44:47], s65 offen offset:2048 nt lds\n\t"
    "buffer_load_dwordx4 v51, s[44:47], s65 offen offset:3072 nt lds\n\t"
    "L_sk3_%=:\n\t"
    "s_mov_b32 s64, -1\n\t"
    "s_mov_b64 s[62:63], 0\n\t"
    "v_and_b32 v237, 3, v234\n\t"
    "v_cmp_eq_u32 vcc, 0, v236\n\t"
    "v_mov_b32 v238, 0x3f803f80\n\t"
    "s_nop 1\n\t"
    "v_cndmask_b32 v238, 0, v238, vcc\n\t"
    "v_accvgpr_write_b32 a224, v238\n\t"
    "v_accvgpr_write_b32 a225, v238\n\t"
    "v_accvgpr_write_b32 a226, v238\n\t"
    "v_accvgpr_write_b32 a227, v238\n\t"
    "v_accvgpr_write_b32 a228, 0\n\t"
    "v_accvgpr_write_b32 a229, 0\n\t"
    "v_accvgpr_write_b32 a230, 0\n\t"
    "v_accvgpr_write_b32 a231, 0\n\t"
    "v_accvgpr_write_b32 a232, 0\n\t"
    "v_accvgpr_write_b32 a233, 0\n\t"
    "v_accvgpr_write_b32 a234, 0\n\t"
    "v_accvgpr_write_b32 a235, 0\n\t"
    "v_accvgpr_write_b32 a236, 0\n\t"
    "v_accvgpr_write_b32 a237, 0\n\t"
    "v_accvgpr_write_b32 a238, 0\n\t"
    "v_accvgpr_write_b32 a239, 0\n\t"
    "v_accvgpr_write_b32 a240, 0\n\t"
    "v_accvgpr_write_b32 a241, 0\n\t"
    "v_accvgpr_write_b32 a242, 0\n\t"
    "v_accvgpr_write_b32 a243, 0\n\t"
    "v_accvgpr_write_b32 a208, 0\n\t"
    "v_accvgpr_write_b32 a209, 0\n\t"
    "v_accvgpr_write_b32 a210, 0\n\t"
    "v_accvgpr_write_b32 a211, 0\n\t"
    "s_lshl_b32 s61, %3, 14\n\t"
    "s_add_u32 s60, s61, 65536\n\t"
    "v_xor_b32 v32, v236, v235\n\t"
    "v_lshlrev_b32 v32, 4, v32\n\t"
    "v_lshl_add_u32 v32, v236, 8, v32\n\t"
    "v_add_u32 v32, s60, v32\n\t"
    "v_xor_b32 v33, v236, v235\n\t"
    "v_xor_b32 v33, 4, v33\n\t"
    "v_lshlrev_b32 v33, 4, v33\n\t"
    "v_lshl_add_u32 v33, v236, 8, v33\n\t"
    "v_add_u32 v33, s60, v33\n\t"
    "v_xor_b32 v34, v236, v235\n\t"
    "v_xor_b32 v34, 8, v34\n\t"
    "v_lshlrev_b32 v34, 4, v34\n\t"
    "v_lshl_add_u32 v34, v236, 8, v34\n\t"
    "v_add_u32 v34, s60, v34\n\t"
    "v_xor_b32 v35, v236, v235\n\t"
    "v_xor_b32 v35, 12, v35\n\t"
    "v_lshlrev_b32 v35, 4, v35\n\t"
    "v_lshl_add_u32 v35, v236, 8, v35\n\t"
    "v_add_u32 v35, s60, v35\n\t"
    "s_add_u32 s56, s61, 65536\n\t"
    "s_add_u32 s57, s61, 65536\n\t"
    "s_mov_b32 s59, 0\n\t"
    "s_lshl_b32 s61, s55, 2\n\t"
    "s_add_u32 s61, s61, 132608\n\t"
    "v_mov_b32 v46, s61\n\t"
    "s_cmp_lt_u32 %3, 2\n\t"
    "s_cbranch_scc0 L_sk4_%=\n\t"
    "v_bfe_u32 v44, v234, 2, 2\n\t"
    "v_lshlrev_b32 v44, 7, v44\n\t"
    "v_lshl_add_u32 v44, v235, 4, v44\n\t"
    "v_lshl_add_u32 v44, v237, 2, v44\n\t"
    "v_add_u32 v44, 131072, v44\n\t"
    "v_add_u32 v239, 132096, v239\n\t"
    "s_waitcnt vmcnt(12)\n\t"
    "v_lshlrev_b32 v160, 16, v226\n\t"
    "v_and_b32 v161, 0xffff0000, v226\n\t"
    "v_lshlrev_b32 v162, 16, v227\n\t"
    "v_and_b32 v163, 0xffff0000, v227\n\t"
    "v_lshlrev_b32 v164, 16, v228\n\t"
    "v_and_b32 v165, 0xffff0000, v228\n\t"
    "v_lshlrev_b32 v166, 16, v229\n\t"
    "v_and_b32 v167, 0xffff0000, v229\n\t"
    "v_mul_f32 v160, 0x3d8293ee, v160\n\t"
    "v_mul_f32 v161, 0x3d8293ee, v161\n\t"
    "v_mul_f32 v162, 0x3d8293ee, v162\n\t"
    "v_mul_f32 v163, 0x3d8293ee, v163\n\t"
    "v_xor_b32 v229, 0x80008000, v229\n\t"
    "ds_write_b32 v44, v160\n\t"
    "ds_write_b32 v44, v161 offset:64\n\t"
    "ds_write_b32 v44, v162 offset:512\n\t"
    "ds_write_b32 v44, v163 offset:576\n\t"
    "ds_write_b32 v239, v228\n\t"
    "ds_write_b32 v239, v229 offset:256\n\t"
    "v_add_f32 v160, |v160|, |v164|\n\t"
    "v_add_f32 v161, |v161|, |v165|\n\t"
    "v_add_f32 v162, |v162|, |v166|\n\t"
    "v_add_f32 v163, |v163|, |v167|\n\t"
    "v_add_f32 v160, v160, v161\n\t"
    "v_add_f32 v162, v162, v163\n\t"
    "v_add_f32 v160, v160, v162\n\t"
    "v_cmp_ngt_f32 vcc, 0x4d800000, v160\n\t"
    "s_cmp_lg_u64 vcc, 0\n\t"
    "s_cselect_b32 s60, 256, 0\n\t"
    "s_or_b64 s[62:63], s[62:63], vcc\n\t"
    "v_mov_b32 v47, s60\n\t"
    "s_waitcnt lgkmcnt(0)\n\t"
    "L_sk4_%=:\n\t"
    "s_cmp_lt_u32 %3, 2\n\t"
    "s_cbranch_scc1 L_sk5_%=\n\t"
    "s_add_u32 s61, s28, 63\n\t"
    "s_and_b32 s61, s61, 63\n\t"
    "s_lshl_b32 s61, s61, 14\n\t"
    "s_lshl_b32 s60, s55, 12\n\t"
    "s_add_u32 s61, s61, s60\n\t"
    "s_lshl_b32 s65, s28, 14\n\t"
    "s_lshl_b32 s60, s55, 9\n\t"
    "s_add_u32 s65, s65, s60\n\t"
    "s_add_u32 s65, s65, 8192\n\t"
    "v_and_b32 v44, 0x1f0, v241\n\t"
    "v_lshrrev_b32 v237, 5, v234\n\t"
    "v_lshl_add_u32 v44, v237, 12, v44\n\t"
    "v_mov_b32 v191, 0\n\t"
    "s_waitcnt vmcnt(17)\n\t"
    "v_lshlrev_b32 v104, 16, v96\n\t"
    "v_and_b32 v105, 0xffff0000, v96\n\t"
    "v_lshlrev_b32 v112, 16, v100\n\t"
    "v_and_b32 v113, 0xffff0000, v100\n\t"
    "v_lshlrev_b32 v106, 16, v97\n\t"
    "v_and_b32 v107, 0xffff0000, v97\n\t"
    "v_lshlrev_b32 v114, 16, v101\n\t"
    "v_and_b32 v115, 0xffff0000, v101\n\t"
    "v_lshlrev_b32 v108, 16, v98\n\t"
    "v_and_b32 v109, 0xffff0000, v98\n\t"
    "v_lshlrev_b32 v116, 16, v102\n\t"
    "v_and_b32 v117, 0xffff0000, v102\n\t"
    "v_lshlrev_b32 v110, 16, v99\n\t"
    "v_and_b32 v111, 0xffff0000, v99\n\t"
    "v_lshlrev_b32 v118, 16, v103\n\t"
    "v_and_b32 v119, 0xffff0000, v103\n\t"
    "s_waitcnt vmcnt(16)\n\t"
    "v_or3_b32 v191, v64, v65, v191\n\t"
    "v_or3_b32 v191, v66, v67, v191\n\t"
    "v_cvt_f32_i32 v64, v64\n\t"
    "v_cvt_f32_i32 v65, v65\n\t"
    "v_cvt_f32_i32 v66, v66\n\t"
    "v_cvt_f32_i32 v67, v67\n\t"
    "v_pk_add_f32 v[64:65], v[64:65], v[104:105] op_sel:[0,0] op_sel_hi:[1,0] neg_lo:[0,1] neg_hi:[0,1]\n\t"
    "v_pk_add_f32 v[66:67], v[66:67], v[104:105] op_sel:[0,0] op_sel_hi:[1,0] neg_lo:[0,1] neg_hi:[0,1]\n\t"
    "v_cvt_pk_bf16_f32 v65, v64, v65\n\t"
    "v_cvt_pk_bf16_f32 v67, v66, v67\n\t"
    "v_lshlrev_b32 v64, 16, v65\n\t"
    "v_and_b32 v65, 0xffff0000, v65\n\t"
    "v_lshlrev_b32 v66, 16, v67\n\t"
    "v_and_b32 v67, 0xffff0000, v67\n\t"
    "v_pk_mul_f32 v[64:65], v[64:65], v[112:113] op_sel:[0,0] op_sel_hi:[1,0]\n\t"
    "v_pk_mul_f32 v[66:67], v[66:67], v[112:113] op_sel:[0,0] op_sel_hi:[1,0]\n\t"
    "s_waitcnt vmcnt(15)\n\t"
    "v_or3_b32 v191, v68, v69, v191\n\t"
    "v_or3_b32 v191, v70, v71, v191\n\t"
    "v_cvt_f32_i32 v68, v68\n\t"
    "v_cvt_f32_i32 v69, v69\n\t"
    "v_cvt_f32_i32 v70, v70\n\t"
    "v_cvt_f32_i32 v71, v71\n\t"
    "v_pk_add_f32 v[68:69], v[68:69], v[104:105] op_sel:[0,1] op_sel_hi:[1,1] neg_lo:[0,1] neg_hi:[0,1]\n\t"
    "v_pk_add_f32 v[70:71], v[70:71], v[104:105] op_sel:[0,1] op_sel_hi:[1,1] neg_lo:[0,1] neg_hi:[0,1]\n\t"
    "v_cvt_pk_bf16_f32 v69, v68, v69\n\t"
    "v_cvt_pk_bf16_f32 v71, v70, v71\n\t"
    "v_lshlrev_b32 v68, 16, v69\n\t"
    "v_and_b32 v69, 0xffff0000, v69\n\t"
    "v_lshlrev_b32 v70, 16, v71\n\t"
    "v_and_b32 v71, 0xffff0000, v71\n\t"
    "v_pk_mul_f32 v[68:69], v[68:69], v[112:113] op_sel:[0,1] op_sel_hi:[1,1]\n\t"
    "v_pk_mul_f32 v[70:71], v[70:71], v[112:113] op_sel:[0,1] op_sel_hi:[1,1]\n\t"
    "v_cvt_pk_bf16_f32 v128, v64, v68\n\t"
    "v_cvt_pk_bf16_f32 v132, v65, v69\n\t"
    "v_cvt_pk_bf16_f32 v136, v66, v70\n\t"
    "v_cvt_pk_bf16_f32 v140, v67, v71\n\t"
    "s_waitcnt vmcnt(14)\n\t"
    "v_or3_b32 v191, v72, v73, v191\n\t"
    "v_or3_b32 v191, v74, v75, v191\n\t"
    "v_cvt_f32_i32 v72, v72\n\t"
    "v_cvt_f32_i32 v73, v73\n\t"
    "v_cvt_f32_i32 v74, v74\n\t"
    "v_cvt_f32_i32 v75, v75\n\t"
    "v_pk_add_f32 v[72:73], v[72:73], v[106:107] op_sel:[0,0] op_sel_hi:[1,0] neg_lo:[0,1] neg_hi:[0,1]\n\t"
    "v_pk_add_f32 v[74:75], v[74:75], v[106:107] op_sel:[0,0] op_sel_hi:[1,0] neg_lo:[0,1] neg_hi:[0,1]\n\t"
    "v_cvt_pk_bf16_f32 v73, v72, v73\n\t"
    "v_cvt_pk_bf16_f32 v75, v74, v75\n\t"
    "v_lshlrev_b32 v72, 16, v73\n\t"
    "v_and_b32 v73, 0xffff0000, v73\n\t"
    "v_lshlrev_b32 v74, 16, v75\n\t"
    "v_and_b32 v75, 0xffff0000, v75\n\t"
    "v_pk_mul_f32 v[72:73], v[72:73], v[114:115] op_sel:[0,0] op_sel_hi:[1,0]\n\t"
    "v_pk_mul_f32 v[74:75], v[74:75], v[114:115] op_sel:[0,0] op_sel_hi:[1,0]\n\t"
    "s_waitcnt vmcnt(13)\n\t"
    "v_or3_b32 v191, v76, v77, v191\n\t"
    "v_or3_b32 v191, v78, v79, v191\n\t"
    "v_cvt_f32_i32 v76, v76\n\t"
    "v_cvt_f32_i32 v77, v77\n\t"
    "v_cvt_f32_i32 v78, v78\n\t"
    "v_cvt_f32_i32 v79, v79\n\t"
    "v_pk_add_f32 v[76:77], v[76:77], v[106:107] op_sel:[0,1] op_sel_hi:[1,1] neg_lo:[0,1] neg_hi:[0,1]\n\t"
    "v_pk_add_f32 v[78:79], v[78:79], v[106:107] op_sel:[0,1] op_sel_hi:[1,1] neg_lo:[0,1] neg_hi:[0,1]\n\t"
    "v_cvt_pk_bf16_f32 v77, v76, v77\n\t"
    "v_cvt_pk_bf16_f32 v79, v78, v79\n\t"
    "v_lshlrev_b32 v76, 16, v77\n\t"
    "v_and_b32 v77, 0xffff0000, v77\n\t"
    "v_lshlrev_b32 v78, 16, v79\n\t"
    "v_and_b32 v79, 0xffff0000, v79\n\t"
    "v_pk_mul_f32 v[76:77], v[76:77], v[114:115] op_sel:[0,1] op_sel_hi:[1,1]\n\t"
    "v_pk_mul_f32 v[78:79], v[78:79], v[114:115] op_sel:[0,1] op_sel_hi:[1,1]\n\t"
    "v_cvt_pk_bf16_f32 v129, v72, v76\n\t"
    "v_cvt_pk_bf16_f32 v133, v73, v77\n\t"
    "v_cvt_pk_bf16_f32 v137, v74, v78\n\t"
    "v_cvt_pk_bf16_f32 v141, v75, v79\n\t"
    "s_waitcnt vmcnt(12)\n\t"
    "v_or3_b32 v191, v80, v81, v191\n\t"
    "v_or3_b32 v191, v82, v83, v191\n\t"
    "v_cvt_f32_i32 v80, v80\n\t"
    "v_cvt_f32_i32 v81, v81\n\t"
    "v_cvt_f32_i32 v82, v82\n\t"
    "v_cvt_f32_i32 v83, v83\n\t"
    "v_pk_add_f32 v[80:81], v[80:81], v[108:109] op_sel:[0,0] op_sel_hi:[1,0] neg_lo:[0,1] neg_hi:[0,1]\n\t"
    "v_pk_add_f32 v[82:83], v[82:83], v[108:109] op_sel:[0,0] op_sel_hi:[1,0] neg_lo:[0,1] neg_hi:[0,1]\n\t"
    "v_cvt_pk_bf16_f32 v81, v80, v81\n\t"
    "v_cvt_pk_bf16_f32 v83, v82, v83\n\t"
    "v_lshlrev_b32 v80, 16, v81\n\t"
    "v_and_b32 v81, 0xffff0000, v81\n\t"
    "v_lshlrev_b32 v82, 16, v83\n\t"
    "v_and_b32 v83, 0xffff0000, v83\n\t"
    "v_pk_mul_f32 v[80:81], v[80:81], v[116:117] op_sel:[0,0] op_sel_hi:[1,0]\n\t"
    "v_pk_mul_f32 v[82:83], v[82:83], v[116:117] op_sel:[0,0] op_sel_hi:[1,0]\n\t"
    "s_waitcnt vmcnt(11)\n\t"
    "v_or3_b32 v191, v84, v85, v191\n\t"
    "v_or3_b32 v191, v86, v87, v191\n\t"
    "v_cvt_f32_i32 v84, v84\n\t"
    "v_cvt_f32_i32 v85, v85\n\t"
    "v_cvt_f32_i32 v86, v86\n\t"
    "v_cvt_f32_i32 v87, v87\n\t"
    "v_pk_add_f32 v[84:85], v[84:85], v[108:109] op_sel:[0,1] op_sel_hi:[1,1] neg_lo:[0,1] neg_hi:[0,1]\n\t"
    "v_pk_add_f32 v[86:87], v[86:87], v[108:109] op_sel:[0,1] op_sel_hi:[1,1] neg_lo:[0,1] neg_hi:[0,1]\n\t"
    "v_cvt_pk_bf16_f32 v85, v84, v85\n\t"
    "v_cvt_pk_bf16_f32 v87, v86, v87\n\t"
    "v_lshlrev_b32 v84, 16, v85\n\t"
    "v_and_b32 v85, 0xffff0000, v85\n\t"
    "v_lshlrev_b32 v86, 16, v87\n\t"
    "v_and_b32 v87, 0xffff0000, v87\n\t"
    "v_pk_mul_f32 v[84:85], v[84:85], v[116:117] op_sel:[0,1] op_sel_hi:[1,1]\n\t"
    "v_pk_mul_f32 v[86:87], v[86:87], v[116:117] op_sel:[0,1] op_sel_hi:[1,1]\n\t"
    "v_cvt_pk_bf16_f32 v130, v80, v84\n\t"
    "v_cvt_pk_bf16_f32 v134, v81, v85\n\t"
    "v_cvt_pk_bf16_f32 v138, v82, v86\n\t"
    "v_cvt_pk_bf16_f32 v142, v83, v87\n\t"
    "s_waitcnt vmcnt(10)\n\t"
    "v_or3_b32 v191, v88, v89, v191\n\t"
    "v_or3_b32 v191, v90, v91, v191\n\t"
    "v_cvt_f32_i32 v88, v88\n\t"
    "v_cvt_f32_i32 v89, v89\n\t"
    "v_cvt_f32_i32 v90, v90\n\t"
    "v_cvt_f32_i32 v91, v91\n\t"
    "v_pk_add_f32 v[88:89], v[88:89], v[110:111] op_sel:[0,0] op_sel_hi:[1,0] neg_lo:[0,1] neg_hi:[0,1]\n\t"
    "v_pk_add_f32 v[90:91], v[90:91], v[110:111] op_sel:[0,0] op_sel_hi:[1,0] neg_lo:[0,1] neg_hi:[0,1]\n\t"
    "v_cvt_pk_bf16_f32 v89, v88, v89\n\t"
    "v_cvt_pk_bf16_f32 v91, v90, v91\n\t"
    "v_lshlrev_b32 v88, 16, v89\n\t"
    "v_and_b32 v89, 0xffff0000, v89\n\t"
    "v_lshlrev_b32 v90, 16, v91\n\t"
    "v_and_b32 v91, 0xffff0000, v91\n\t"
    "v_pk_mul_f32 v[88:89], v[88:89], v[118:119] op_sel:[0,0] op_sel_hi:[1,0]\n\t"
    "v_pk_mul_f32 v[90:91], v[90:91], v[118:119] op_sel:[0,0] op_sel_hi:[1,0]\n\t"
    "s_waitcnt vmcnt(9)\n\t"
    "v_or3_b32 v191, v92, v93, v191\n\t"
    "v_or3_b32 v191, v94, v95, v191\n\t"
    "v_cvt_f32_i32 v92, v92\n\t"
    "v_cvt_f32_i32 v93, v93\n\t"
    "v_cvt_f32_i32 v94, v94\n\t"
    "v_cvt_f32_i32 v95, v95\n\t"
    "v_pk_add_f32 v[92:93], v[92:93], v[110:111] op_sel:[0,1] op_sel_hi:[1,1] neg_lo:[0,1] neg_hi:[0,1]\n\t"
    "v_pk_add_f32 v[94:95], v[94:95], v[110:111] op_sel:[0,1] op_sel_hi:[1,1] neg_lo:[0,1] neg_hi:[0,1]\n\t"
    "v_cvt_pk_bf16_f32 v93, v92, v93\n\t"
    "v_cvt_pk_bf16_f32 v95, v94, v95\n\t"
    "v_lshlrev_b32 v92, 16, v93\n\t"
    "v_and_b32 v93, 0xffff0000, v93\n\t"
    "v_lshlrev_b32 v94, 16, v95\n\t"
    "v_and_b32 v95, 0xffff0000, v95\n\t"
    "v_pk_mul_f32 v[92:93], v[92:93], v[118:119] op_sel:[0,1] op_sel_hi:[1,1]\n\t"
    "v_pk_mul_f32 v[94:95], v[94:95], v[118:119] op_sel:[0,1] op_sel_hi:[1,1]\n\t"
    "v_cvt_pk_bf16_f32 v131, v88, v92\n\t"
    "v_cvt_pk_bf16_f32 v135, v89, v93\n\t"
    "v_cvt_pk_bf16_f32 v139, v90, v94\n\t"
    "v_cvt_pk_bf16_f32 v143, v91, v95\n\t"
    "buffer_store_dwordx4 v[128:131], v44, s[40:43], s65 offen offset:0 sc1\n\t"
    "buffer_store_dwordx4 v[132:135], v44, s[40:43], s65 offen offset:1024 sc1\n\t"
    "buffer_store_dwordx4 v[136:139], v44, s[40:43], s65 offen offset:2048 sc1\n\t"
    "buffer_store_dwordx4 v[140:143], v44, s[40:43], s65 offen offset:3072 sc1\n\t"
    "s_waitcnt vmcnt(11)\n\t"
    "v_or3_b32 v191, v0, v1, v191\n\t"
    "v_or3_b32 v191, v2, v3, v191\n\t"
    "v_or3_b32 v191, v4, v5, v191\n\t"
    "v_or3_b32 v191, v6, v7, v191\n\t"
    "v_cvt_f32_i32 v0, v0\n\t"
    "v_cvt_f32_i32 v1, v1\n\t"
    "v_cvt_f32_i32 v2, v2\n\t"
    "v_cvt_f32_i32 v3, v3\n\t"
    "v_cvt_f32_i32 v4, v4\n\t"
    "v_cvt_f32_i32 v5, v5\n\t"
    "v_cvt_f32_i32 v6, v6\n\t"
    "v_cvt_f32_i32 v7, v7\n\t"
    "v_pk_add_f32 v[0:1], v[0:1], v[0:1]\n\t"
    "v_pk_add_f32 v[2:3], v[2:3], v[2:3]\n\t"
    "v_pk_add_f32 v[4:5], v[4:5], v[4:5]\n\t"
    "v_pk_add_f32 v[6:7], v[6:7], v[6:7]\n\t"
    "v_cvt_pk_bf16_f32 v160, v0, v1\n\t"
    "v_cvt_pk_bf16_f32 v161, v2, v3\n\t"
    "v_cvt_pk_bf16_f32 v162, v4, v5\n\t"
    "v_cvt_pk_bf16_f32 v163, v6, v7\n\t"
    "buffer_store_dwordx4 v[160:163], v241, s[40:43], s61 offen offset:0 sc1\n\t"
    "s_waitcnt vmcnt(10)\n\t"
    "v_or3_b32 v191, v8, v9, v191\n\t"
    "v_or3_b32 v191, v10, v11, v191\n\t"
    "v_or3_b32 v191, v12, v13, v191\n\t"
    "v_or3_b32 v191, v14, v15, v191\n\t"
    "v_cvt_f32_i32 v8, v8\n\t"
    "v_cvt_f32_i32 v9, v9\n\t"
    "v_cvt_f32_i32 v10, v10\n\t"
    "v_cvt_f32_i32 v11, v11\n\t"
    "v_cvt_f32_i32 v12, v12\n\t"
    "v_cvt_f32_i32 v13, v13\n\t"
    "v_cvt_f32_i32 v14, v14\n\t"
    "v_cvt_f32_i32 v15, v15\n\t"
    "v_pk_add_f32 v[8:9], v[8:9], v[8:9]\n\t"
    "v_pk_add_f32 v[10:11], v[10:11], v[10:11]\n\t"
    "v_pk_add_f32 v[12:13], v[12:13], v[12:13]\n\t"
    "v_pk_add_f32 v[14:15], v[14:15], v[14:15]\n\t"
    "v_cvt_pk_bf16_f32 v164, v8, v9\n\t"
    "v_cvt_pk_bf16_f32 v165, v10, v11\n\t"
    "v_cvt_pk_bf16_f32 v166, v12, v13\n\t"
    "v_cvt_pk_bf16_f32 v167, v14, v15\n\t"
    "buffer_store_dwordx4 v[164:167], v241, s[40:43], s61 offen offset:1024 sc1\n\t"
    "s_waitcnt vmcnt(9)\n\t"
    "v_or3_b32 v191, v16, v17, v191\n\t"
    "v_or3_b32 v191, v18, v19, v191\n\t"
    "v_or3_b32 v191, v20, v21, v191\n\t"
    "v_or3_b32 v191, v22, v23, v191\n\t"
    "v_cvt_f32_i32 v16, v16\n\t"
    "v_cvt_f32_i32 v17, v17\n\t"
    "v_cvt_f32_i32 v18, v18\n\t"
    "v_cvt_f32_i32 v19, v19\n\t"
    "v_cvt_f32_i32 v20, v20\n\t"
    "v_cvt_f32_i32 v21, v21\n\t"
    "v_cvt_f32_i32 v22, v22\n\t"
    "v_cvt_f32_i32 v23, v23\n\t"
    "v_pk_add_f32 v[16:17], v[16:17], v[16:17]\n\t"
    "v_pk_add_f32 v[18:19], v[18:19], v[18:19]\n\t"
    "v_pk_add_f32 v[20:21], v[20:21], v[20:21]\n\t"
    "v_pk_add_f32 v[22:23], v[22:23], v[22:23]\n\t"
    "v_cvt_pk_bf16_f32 v168, v16, v17\n\t"
    "v_cvt_pk_bf16_f32 v169, v18, v19\n\t"
    "v_cvt_pk_bf16_f32 v170, v20, v21\n\t"
    "v_cvt_pk_bf16_f32 v171, v22, v23\n\t"
    "buffer_store_dwordx4 v[168:171], v241, s[40:43], s61 offen offset:2048 sc1\n\t"
    "s_waitcnt vmcnt(8)\n\t"
    "v_or3_b32 v191, v24, v25, v191\n\t"
    "v_or3_b32 v191, v26, v27, v191\n\t"
    "v_or3_b32 v191, v28, v29, v191\n\t"
    "v_or3_b32 v191, v30, v31, v191\n\t"
    "v_cvt_f32_i32 v24, v24\n\t"
    "v_cvt_f32_i32 v25, v25\n\t"
    "v_cvt_f32_i32 v26, v26\n\t"
    "v_cvt_f32_i32 v27, v27\n\t"
    "v_cvt_f32_i32 v28, v28\n\t"
    "v_cvt_f32_i32 v29, v29\n\t"
    "v_cvt_f32_i32 v30, v30\n\t"
    "v_cvt_f32_i32 v31, v31\n\t"
    "v_pk_add_f32 v[24:25], v[24:25], v[24:25]\n\t"
    "v_pk_add_f32 v[26:27], v[26:27], v[26:27]\n\t"
    "v_pk_add_f32 v[28:29], v[28:29], v[28:29]\n\t"
    "v_pk_add_f32 v[30:31], v[30:31], v[30:31]\n\t"
    "v_cvt_pk_bf16_f32 v172, v24, v25\n\t"
    "v_cvt_pk_bf16_f32 v173, v26, v27\n\t"
    "v_cvt_pk_bf16_f32 v174, v28, v29\n\t"
    "v_cvt_pk_bf16_f32 v175, v30, v31\n\t"
    "buffer_store_dwordx4 v[172:175], v241, s[40:43], s61 offen offset:3072 sc1\n\t"
    "v_cmp_lt_u32 vcc, 0xff, v191\n\t"
    "s_or_b64 s[62:63], s[62:63], vcc\n\t"
    "v_add_f32 v104, |v104|, |v112|\n\t"
    "v_add_f32 v105, |v105|, |v113|\n\t"
    "v_add_f32 v106, |v106|, |v114|\n\t"
    "v_add_f32 v107, |v107|, |v115|\n\t"
    "v_add_f32 v108, |v108|, |v116|\n\t"
    "v_add_f32 v109, |v109|, |v117|\n\t"
    "v_add_f32 v110, |v110|, |v118|\n\t"
    "v_add_f32 v111, |v111|, |v119|\n\t"
    "v_add_f32 v104, v104, v108\n\t"
    "v_add_f32 v105, v105, v109\n\t"
    "v_add_f32 v106, v106, v110\n\t"
    "v_add_f32 v107, v107, v111\n\t"
    "v_add_f32 v104, v104, v105\n\t"
    "v_add_f32 v106, v106, v107\n\t"
    "v_add_f32 v104, v104, v106\n\t"
    "v_cmp_ngt_f32 vcc, 0x47800000, v104\n\t"
    "s_or_b64 s[62:63], s[62:63], vcc\n\t"
    "s_waitcnt vmcnt(8)\n\t"
    "v_readfirstlane_b32 s52, v224\n\t"
    "v_readfirstlane_b32 s53, v225\n\t"
    "s_mul_i32 s66, s52, 0x9e3779b1\n\t"
    "s_add_u32 s66, s66, 0x7f4a7c15\n\t"
    "s_mul_i32 s67, s52, 0x85ebca6b\n\t"
    "s_xor_b32 s67, s67, s53\n\t"
    "s_xor_b32 s67, s67, 0xc2b2ae35\n\t"
    "s_and_b32 s67, s67, -2\n\t"
    "s_cmp_lg_u64 s[62:63], 0\n\t"
    "s_cselect_b32 s60, 1, 0\n\t"
    "s_or_b32 s60, s60, s67\n\t"
    "v_mov_b32 v36, s66\n\t"
    "v_mov_b32 v37, s60\n\t"
    "s_lshl_b32 s60, s28, 4\n\t"
    "s_lshl_b32 s61, s55, 3\n\t"
    "s_add_u32 s60, s60, s61\n\t"
    "v_mov_b32 v38, 1280\n\t"
    "v_mad_u32_u24 v38, v38, v234, s60\n\t"
    "L_sk5_%=:\n\t"
    "v_lshlrev_b32 v238, 7, v235\n\t"
    "v_add_u32 v238, 131072, v238\n\t"
    "v_lshlrev_b32 v239, 4, v235\n\t"
    "v_add_u32 v239, 132096, v239\n\t"
    "s_cmp_lt_u32 %3, 2\n\t"
    "s_cbranch_scc0 L_sk7_%=\n\t"
    "s_waitcnt vmcnt(8)\n\t"
    "v_add_u32 v47, 1, v47\n\t"
    "ds_write_b32 v46, v47\n\t"
    "s_lshl_b32 s60, %3, 14\n\t"
    "s_add_u32 s65, s60, 40960\n\t"
    "s_add_u32 m0, s60, 106496\n\t"
    "s_nop 0\n\t"
    "buffer_load_dwordx4 v48, s[44:47], s65 offen offset:0 nt lds\n\t"
    "buffer_load_dwordx4 v49, s[44:47], s65 offen offset:1024 nt lds\n\t"
    "buffer_load_dwordx4 v50, s[44:47], s65 offen offset:2048 nt lds\n\t"
    "buffer_load_dwordx4 v51, s[44:47], s65 offen offset:3072 nt lds\n\t"
    "s_waitcnt vmcnt(8)\n\t"
    "v_add_u32 v47, 1, v47\n\t"
    "ds_write_b32 v46, v47\n\t"
    "s_lshl_b32 s60, %3, 14\n\t"
    "s_add_u32 s65, s60, 4096\n\t"
    "s_add_u32 m0, s60, 69632\n\t"
    "s_nop 0\n\t"
    "buffer_load_dwordx4 v48, s[44:47], s65 offen offset:0 nt lds\n\t"
    "buffer_load_dwordx4 v49, s[44:47], s65 offen offset:1024 nt lds\n\t"
    "buffer_load_dwordx4 v50, s[44:47], s65 offen offset:2048 nt lds\n\t"
    "buffer_load_dwordx4 v51, s[44:47], s65 offen offset:3072 nt lds\n\t"
    "s_waitcnt vmcnt(8)\n\t"
    "s_lshl_b32 s60, %3, 14\n\t"
    "s_add_u32 s65, s60, 45056\n\t"
    "s_add_u32 m0, s60, 110592\n\t"
    "s_nop 0\n\t"
    "buffer_load_dwordx4 v48, s[44:47], s65 offen offset:0 nt lds\n\t"
    "buffer_load_dwordx4 v49, s[44:47], s65 offen offset:1024 nt lds\n\t"
    "buffer_load_dwordx4 v50, s[44:47], s65 offen offset:2048 nt lds\n\t"
    "buffer_load_dwordx4 v51, s[44:47], s65 offen offset:3072 nt lds\n\t"
    "L_sk7_%=:\n\t"
    "s_cmp_lt_u32 %3, 2\n\t"
    "s_cbranch_scc1 L_sk8_%=\n\t"
    "L_wq6_%=:\n\t"
    "ds_read_b32 v41, v46\n\t"
    "s_waitcnt lgkmcnt(0)\n\t"
    "v_readfirstlane_b32 s60, v41\n\t"
    "s_and_b32 s61, s60, 255\n\t"
    "s_cmp_le_u32 s61, 0\n\t"
    "s_cbranch_scc1 L_wq6_%=\n\t"
    "s_lshr_b32 s60, s60, 8\n\t"
    "s_or_b32 s62, s62, s60\n\t"
    "L_sk8_%=:\n\t"
    "ds_read_b128 v[208:211], v32 offset:0\n\t"
    "ds_read_b128 v[212:215], v33 offset:0\n\t"
    "ds_read_b128 v[216:219], v34 offset:0\n\t"
    "ds_read_b128 v[220:223], v35 offset:0\n\t"
    "ds_read_b128 v[64:67], v238 offset:0\n\t"
    "ds_read_b128 v[68:71], v238 offset:16\n\t"
    "ds_read_b128 v[72:75], v238 offset:32\n\t"
    "ds_read_b128 v[76:79], v238 offset:48\n\t"
    "ds_read_b128 v[80:83], v238 offset:64\n\t"
    "ds_read_b128 v[84:87], v238 offset:80\n\t"
    "ds_read_b128 v[88:91], v238 offset:96\n\t"
    "ds_read_b128 v[92:95], v238 offset:112\n\t"
    "s_waitcnt lgkmcnt(8)\n\t"
    "ds_read_b128 v[96:99], v238 offset:512\n\t"
    "ds_read_b128 v[100:103], v238 offset:528\n\t"
    "ds_read_b128 v[104:107], v238 offset:544\n\t"
    "ds_read_b128 v[108:111], v238 offset:560\n\t"
    "ds_read_b128 v[112:115], v238 offset:576\n\t"
    "ds_read_b128 v[116:119], v238 offset:592\n\t"
    "v_lshlrev_b32 v128, 16, v208\n\t"
    "v_and_b32 v144, 0xffff0000, v208\n\t"
    "v_lshlrev_b32 v129, 16, v209\n\t"
    "v_and_b32 v145, 0xffff0000, v209\n\t"
    "v_lshlrev_b32 v130, 16, v210\n\t"
    "v_and_b32 v146, 0xffff0000, v210\n\t"
    "v_lshlrev_b32 v131, 16, v211\n\t"
    "v_and_b32 v147, 0xffff0000, v211\n\t"
    "v_lshlrev_b32 v132, 16, v212\n\t"
    "v_and_b32 v148, 0xffff0000, v212\n\t"
    "v_lshlrev_b32 v133, 16, v213\n\t"
    "v_and_b32 v149, 0xffff0000, v213\n\t"
    "v_lshlrev_b32 v134, 16, v214\n\t"
    "v_and_b32 v150, 0xffff0000, v214\n\t"
    "v_lshlrev_b32 v135, 16, v215\n\t"
    "v_and_b32 v151, 0xffff0000, v215\n\t"
    "v_lshlrev_b32 v136, 16, v216\n\t"
    "v_and_b32 v152, 0xffff0000, v216\n\t"
    "v_lshlrev_b32 v137, 16, v217\n\t"
    "v_and_b32 v153, 0xffff0000, v217\n\t"
    "v_lshlrev_b32 v138, 16, v218\n\t"
    "v_and_b32 v154, 0xffff0000, v218\n\t"
    "v_lshlrev_b32 v139, 16, v219\n\t"
    "v_and_b32 v155, 0xffff0000, v219\n\t"
    "v_lshlrev_b32 v140, 16, v220\n\t"
    "v_and_b32 v156, 0xffff0000, v220\n\t"
    "v_lshlrev_b32 v141, 16, v221\n\t"
    "v_and_b32 v157, 0xffff0000, v221\n\t"
    "v_lshlrev_b32 v142, 16, v222\n\t"
    "v_and_b32 v158, 0xffff0000, v222\n\t"
    "v_lshlrev_b32 v143, 16, v223\n\t"
    "v_and_b32 v159, 0xffff0000, v223\n\t"
    "s_waitcnt lgkmcnt(4)\n\t"
    "ds_read_b128 v[120:123], v238 offset:608\n\t"
    "ds_read_b128 v[124:127], v238 offset:624\n\t"
    "ds_read_b128 a[160:163], v239 offset:0\n\t"
    "ds_read_b128 a[164:167], v239 offset:64\n\t"
    "ds_read_b128 a[168:171], v239 offset:128\n\t"
    "ds_read_b128 a[172:175], v239 offset:192\n\t"
    "ds_read_b128 a[176:179], v239 offset:256\n\t"
    "ds_read_b128 a[180:183], v239 offset:320\n\t"
    "ds_read_b128 a[184:187], v239 offset:384\n\t"
    "ds_read_b128 a[188:191], v239 offset:448\n\t"
    "v_pk_mul_f32 v[160:161], v[128:129], v[64:65]\n\t"
    "v_pk_mul_f32 v[162:163], v[130:131], v[66:67]\n\t"
    "v_pk_mul_f32 v[164:165], v[132:133], v[68:69]\n\t"
    "v_pk_mul_f32 v[166:167], v[134:135], v[70:71]\n\t"
    "v_pk_mul_f32 v[168:169], v[136:137], v[72:73]\n\t"
    "v_pk_mul_f32 v[170:171], v[138:139], v[74:75]\n\t"
    "v_pk_mul_f32 v[172:173], v[140:141], v[76:77]\n\t"
    "v_pk_mul_f32 v[174:175], v[142:143], v[78:79]\n\t"
    "v_pk_mul_f32 v[176:177], v[144:145], v[80:81]\n\t"
    "v_pk_mul_f32 v[178:179], v[146:147], v[82:83]\n\t"
    "v_pk_mul_f32 v[180:181], v[148:149], v[84:85]\n\t"
    "v_pk_mul_f32 v[182:183], v[150:151], v[86:87]\n\t"
    "v_pk_mul_f32 v[184:185], v[152:153], v[88:89]\n\t"
    "v_pk_mul_f32 v[186:187], v[154:155], v[90:91]\n\t"
    "v_pk_mul_f32 v[188:189], v[156:157], v[92:93]\n\t"
    "v_pk_mul_f32 v[190:191], v[158:159], v[94:95]\n\t"
    "v_cvt_pk_bf16_f32 v0, v160, v176\n\t"
    "v_cvt_pk_bf16_f32 v1, v161, v177\n\t"
    "v_cvt_pk_bf16_f32 v2, v162, v178\n\t"
    "v_cvt_pk_bf16_f32 v3, v163, v179\n\t"
    "v_cvt_pk_bf16_f32 v4, v164, v180\n\t"
    "v_cvt_pk_bf16_f32 v5, v165, v181\n\t"
    "v_cvt_pk_bf16_f32 v6, v166, v182\n\t"
    "v_cvt_pk_bf16_f32 v7, v167, v183\n\t"
    "v_cvt_pk_bf16_f32 v8, v168, v184\n\t"
    "v_cvt_pk_bf16_f32 v9, v169, v185\n\t"
    "v_cvt_pk_bf16_f32 v10, v170, v186\n\t"
    "v_cvt_pk_bf16_f32 v11, v171, v187\n\t"
    "v_cvt_pk_bf16_f32 v12, v172, v188\n\t"
    "v_cvt_pk_bf16_f32 v13, v173, v189\n\t"
    "v_cvt_pk_bf16_f32 v14, v174, v190\n\t"
    "v_cvt_pk_bf16_f32 v15, v175, v191\n\t"
    "s_waitcnt lgkmcnt(0)\n\t"
    "v_pk_mul_f32 v[160:161], v[128:129], v[96:97]\n\t"
    "v_pk_mul_f32 v[162:163], v[130:131], v[98:99]\n\t"
    "v_pk_mul_f32 v[164:165], v[132:133], v[100:101]\n\t"
    "v_pk_mul_f32 v[166:167], v[134:135], v[102:103]\n\t"
    "v_pk_mul_f32 v[168:169], v[136:137], v[104:105]\n\t"
    "v_pk_mul_f32 v[170:171], v[138:139], v[106:107]\n\t"
    "v_pk_mul_f32 v[172:173], v[140:141], v[108:109]\n\t"
    "v_pk_mul_f32 v[174:175], v[142:143], v[110:111]\n\t"
    "v_pk_mul_f32 v[176:177], v[144:145], v[112:113]\n\t"
    "v_pk_mul_f32 v[178:179], v[146:147], v[114:115]\n\t"
    "v_pk_mul_f32 v[180:181], v[148:149], v[116:117]\n\t"
    "v_pk_mul_f32 v[182:183], v[150:151], v[118:119]\n\t"
    "v_pk_mul_f32 v[184:185], v[152:153], v[120:121]\n\t"
    "v_pk_mul_f32 v[186:187], v[154:155], v[122:123]\n\t"
    "v_pk_mul_f32 v[188:189], v[156:157], v[124:125]\n\t"
    "v_pk_mul_f32 v[190:191], v[158:159], v[126:127]\n\t"
    "v_cvt_pk_bf16_f32 v192, v160, v176\n\t"
    "v_cvt_pk_bf16_f32 v193, v161, v177\n\t"
    "v_cvt_pk_bf16_f32 v194, v162, v178\n\t"
    "v_cvt_pk_bf16_f32 v195, v163, v179\n\t"
    "v_cvt_pk_bf16_f32 v196, v164, v180\n\t"
    "v_cvt_pk_bf16_f32 v197, v165, v181\n\t"
    "v_cvt_pk_bf16_f32 v198, v166, v182\n\t"
    "v_cvt_pk_bf16_f32 v199, v167, v183\n\t"
    "v_cvt_pk_bf16_f32 v200, v168, v184\n\t"
    "v_cvt_pk_bf16_f32 v201, v169, v185\n\t"
    "v_cvt_pk_bf16_f32 v202, v170, v186\n\t"
    "v_cvt_pk_bf16_f32 v203, v171, v187\n\t"
    "v_cvt_pk_bf16_f32 v204, v172, v188\n\t"
    "v_cvt_pk_bf16_f32 v205, v173, v189\n\t"
    "v_cvt_pk_bf16_f32 v206, v174, v190\n\t"
    "v_cvt_pk_bf16_f32 v207, v175, v191\n\t"
    "v_mfma_f32_16x16x32_bf16 a[192:195], a[160:163], v[0:3], 0\n\t"
    "v_accvgpr_write_b32 a0, v0\n\t"
    "v_accvgpr_write_b32 a1, v1\n\t"
    "v_accvgpr_write_b32 a2, v2\n\t"
    "v_accvgpr_write_b32 a3, v3\n\t"
    "v_mfma_f32_16x16x32_bf16 a[192:195], a[164:167], v[4:7], a[192:195]\n\t"
    "v_accvgpr_write_b32 a4, v4\n\t"
    "v_accvgpr_write_b32 a5, v5\n\t"
    "v_accvgpr_write_b32 a6, v6\n\t"
    "v_accvgpr_write_b32 a7, v7\n\t"
    "v_mfma_f32_16x16x32_bf16 a[192:195], a[168:171], v[8:11], a[192:195]\n\t"
    "v_accvgpr_write_b32 a8, v8\n\t"
    "v_accvgpr_write_b32 a9, v9\n\t"
    "v_accvgpr_write_b32 a10, v10\n\t"
    "v_accvgpr_write_b32 a11, v11\n\t"
    "v_mfma_f32_16x16x32_bf16 a[192:195], a[172:175], v[12:15], a[192:195]\n\t"
    "v_accvgpr_write_b32 a12, v12\n\t"
    "v_accvgpr_write_b32 a13, v13\n\t"
    "v_accvgpr_write_b32 a14, v14\n\t"
    "v_accvgpr_write_b32 a15, v15\n\t"
    "v_mfma_f32_16x16x32_bf16 a[192:195], a[176:179], v[192:195], a[192:195]\n\t"
    "v_accvgpr_write_b32 a64, v192\n\t"
    "v_accvgpr_write_b32 a65, v193\n\t"
    "v_accvgpr_write_b32 a66, v194\n\t"
    "v_accvgpr_write_b32 a67, v195\n\t"
    "v_mfma_f32_16x16x32_bf16 a[192:195], a[180:183], v[196:199], a[192:195]\n\t"
    "v_accvgpr_write_b32 a68, v196\n\t"
    "v_accvgpr_write_b32 a69, v197\n\t"
    "v_accvgpr_write_b32 a70, v198\n\t"
    "v_accvgpr_write_b32 a71, v199\n\t"
    "v_mfma_f32_16x16x32_bf16 a[192:195], a[184:187], v[200:203], a[192:195]\n\t"
    "v_accvgpr_write_b32 a72, v200\n\t"
    "v_accvgpr_write_b32 a73, v201\n\t"
    "v_accvgpr_write_b32 a74, v202\n\t"
    "v_accvgpr_write_b32 a75, v203\n\t"
    "v_mfma_f32_16x16x32_bf16 a[192:195], a[188:191], v[204:207], a[192:195]\n\t"
    "v_accvgpr_write_b32 a76, v204\n\t"
    "v_accvgpr_write_b32 a77, v205\n\t"
    "v_accvgpr_write_b32 a78, v206\n\t"
    "v_accvgpr_write_b32 a79, v207\n\t"
    "s_cmp_lt_u32 %3, 2\n\t"
    "s_cbranch_scc0 L_sk10_%=\n\t"
    "s_waitcnt vmcnt(8)\n\t"
    "v_add_u32 v47, 1, v47\n\t"
    "ds_write_b32 v46, v47\n\t"
    "s_lshl_b32 s60, %3, 14\n\t"
    "s_add_u32 s65, s60, 8192\n\t"
    "s_add_u32 m0, s60, 73728\n\t"
    "s_nop 0\n\t"
    "buffer_load_dwordx4 v48, s[44:47], s65 offen offset:0 nt lds\n\t"
    "buffer_load_dwordx4 v49, s[44:47], s65 offen offset:1024 nt lds\n\t"
    "buffer_load_dwordx4 v50, s[44:47], s65 offen offset:2048 nt lds\n\t"
    "buffer_load_dwordx4 v51, s[44:47], s65 offen offset:3072 nt lds\n\t"
    "s_waitcnt vmcnt(8)\n\t"
    "s_lshl_b32 s60, %3, 14\n\t"
    "s_add_u32 s65, s60, 12288\n\t"
    "s_add_u32 m0, s60, 77824\n\t"
    "s_nop 0\n\t"
    "buffer_load_dwordx4 v48, s[44:47], s65 offen offset:0 nt lds\n\t"
    "buffer_load_dwordx4 v49, s[44:47], s65 offen offset:1024 nt lds\n\t"
    "buffer_load_dwordx4 v50, s[44:47], s65 offen offset:2048 nt lds\n\t"
    "buffer_load_dwordx4 v51, s[44:47], s65 offen offset:3072 nt lds\n\t"
    "L_sk10_%=:\n\t"
    "s_cmp_lt_u32 %3, 2\n\t"
    "s_cbranch_scc1 L_sk11_%=\n\t"
    "L_wq9_%=:\n\t"
    "ds_read_b32 v41, v46\n\t"
    "s_waitcnt lgkmcnt(0)\n\t"
    "v_readfirstlane_b32 s60, v41\n\t"
    "s_and_b32 s61, s60, 255\n\t"
    "s_cmp_le_u32 s61, 1\n\t"
    "s_cbranch_scc1 L_wq9_%=\n\t"
    "L_sk11_%=:\n\t"
    "ds_read_b128 v[208:211], v32 offset:4096\n\t"
    "ds_read_b128 v[212:215], v33 offset:4096\n\t"
    "ds_read_b128 v[216:219], v34 offset:4096\n\t"
    "ds_read_b128 v[220:223], v35 offset:4096\n\t"
    "s_waitcnt lgkmcnt(0)\n\t"
    "v_lshlrev_b32 v128, 16, v208\n\t"
    "v_and_b32 v144, 0xffff0000, v208\n\t"
    "v_lshlrev_b32 v129, 16, v209\n\t"
    "v_and_b32 v145, 0xffff0000, v209\n\t"
    "v_lshlrev_b32 v130, 16, v210\n\t"
    "v_and_b32 v146, 0xffff0000, v210\n\t"
    "v_lshlrev_b32 v131, 16, v211\n\t"
    "v_and_b32 v147, 0xffff0000, v211\n\t"
    "v_lshlrev_b32 v132, 16, v212\n\t"
    "v_and_b32 v148, 0xffff0000, v212\n\t"
    "v_lshlrev_b32 v133, 16, v213\n\t"
    "v_and_b32 v149, 0xffff0000, v213\n\t"
    "v_lshlrev_b32 v134, 16, v214\n\t"
    "v_and_b32 v150, 0xffff0000, v214\n\t"
    "v_lshlrev_b32 v135, 16, v215\n\t"
    "v_and_b32 v151, 0xffff0000, v215\n\t"
    "v_lshlrev_b32 v136, 16, v216\n\t"
    "v_and_b32 v152, 0xffff0000, v216\n\t"
    "v_lshlrev_b32 v137, 16, v217\n\t"
    "v_and_b32 v153, 0xffff0000, v217\n\t"
    "v_lshlrev_b32 v138, 16, v218\n\t"
    "v_and_b32 v154, 0xffff0000, v218\n\t"
    "v_lshlrev_b32 v139, 16, v219\n\t"
    "v_and_b32 v155, 0xffff0000, v219\n\t"
    "v_lshlrev_b32 v140, 16, v220\n\t"
    "v_and_b32 v156, 0xffff0000, v220\n\t"
    "v_lshlrev_b32 v141, 16, v221\n\t"
    "v_and_b32 v157, 0xffff0000, v221\n\t"
    "v_lshlrev_b32 v142, 16, v222\n\t"
    "v_and_b32 v158, 0xffff0000, v222\n\t"
    "v_lshlrev_b32 v143, 16, v223\n\t"
    "v_and_b32 v159, 0xffff0000, v223\n\t"
    "v_pk_mul_f32 v[160:161], v[128:129], v[64:65]\n\t"
    "v_pk_mul_f32 v[162:163], v[130:131], v[66:67]\n\t"
    "v_pk_mul_f32 v[164:165], v[132:133], v[68:69]\n\t"
    "v_pk_mul_f32 v[166:167], v[134:135], v[70:71]\n\t"
    "v_pk_mul_f32 v[168:169], v[136:137], v[72:73]\n\t"
    "v_pk_mul_f32 v[170:171], v[138:139], v[74:75]\n\t"
    "v_pk_mul_f32 v[172:173], v[140:141], v[76:77]\n\t"
    "v_pk_mul_f32 v[174:175], v[142:143], v[78:79]\n\t"
    "v_pk_mul_f32 v[176:177], v[144:145], v[80:81]\n\t"
    "v_pk_mul_f32 v[178:179], v[146:147], v[82:83]\n\t"
    "v_pk_mul_f32 v[180:181], v[148:149], v[84:85]\n\t"
    "v_pk_mul_f32 v[182:183], v[150:151], v[86:87]\n\t"
    "v_pk_mul_f32 v[184:185], v[152:153], v[88:89]\n\t"
    "v_pk_mul_f32 v[186:187], v[154:155], v[90:91]\n\t"
    "v_pk_mul_f32 v[188:189], v[156:157], v[92:93]\n\t"
    "v_pk_mul_f32 v[190:191], v[158:159], v[94:95]\n\t"
    "v_cvt_pk_bf16_f32 v0, v160, v176\n\t"
    "v_cvt_pk_bf16_f32 v1, v161, v177\n\t"
    "v_cvt_pk_bf16_f32 v2, v162, v178\n\t"
    "v_cvt_pk_bf16_f32 v3, v163, v179\n\t"
    "v_cvt_pk_bf16_f32 v4, v164, v180\n\t"
    "v_cvt_pk_bf16_f32 v5, v165, v181\n\t"
    "v_cvt_pk_bf16_f32 v6, v166, v182\n\t"
    "v_cvt_pk_bf16_f32 v7, v167, v183\n\t"
    "v_cvt_pk_bf16_f32 v8, v168, v184\n\t"
    "v_cvt_pk_bf16_f32 v9, v169, v185\n\t"
    "v_cvt_pk_bf16_f32 v10, v170, v186\n\t"
    "v_cvt_pk_bf16_f32 v11, v171, v187\n\t"
    "v_cvt_pk_bf16_f32 v12, v172, v188\n\t"
    "v_cvt_pk_bf16_f32 v13, v173, v189\n\t"
    "v_cvt_pk_bf16_f32 v14, v174, v190\n\t"
    "v_cvt_pk_bf16_f32 v15, v175, v191\n\t"
    "v_pk_mul_f32 v[160:161], v[128:129], v[96:97]\n\t"
    "v_pk_mul_f32 v[162:163], v[130:131], v[98:99]\n\t"
    "v_pk_mul_f32 v[164:165], v[132:133], v[100:101]\n\t"
    "v_pk_mul_f32 v[166:167], v[134:135], v[102:103]\n\t"
    "v_pk_mul_f32 v[168:169], v[136:137], v[104:105]\n\t"
    "v_pk_mul_f32 v[170:171], v[138:139], v[106:107]\n\t"
    "v_pk_mul_f32 v[172:173], v[140:141], v[108:109]\n\t"
    "v_pk_mul_f32 v[174:175], v[142:143], v[110:111]\n\t"
    "v_pk_mul_f32 v[176:177], v[144:145], v[112:113]\n\t"
    "v_pk_mul_f32 v[178:179], v[146:147], v[114:115]\n\t"
    "v_pk_mul_f32 v[180:181], v[148:149], v[116:117]\n\t"
    "v_pk_mul_f32 v[182:183], v[150:151], v[118:119]\n\t"
    "v_pk_mul_f32 v[184:185], v[152:153], v[120:121]\n\t"
    "v_pk_mul_f32 v[186:187], v[154:155], v[122:123]\n\t"
    "v_pk_mul_f32 v[188:189], v[156:157], v[124:125]\n\t"
    "v_pk_mul_f32 v[190:191], v[158:159], v[126:127]\n\t"
    "v_cvt_pk_bf16_f32 v192, v160, v176\n\t"
    "v_cvt_pk_bf16_f32 v193, v161, v177\n\t"
    "v_cvt_pk_bf16_f32 v194, v162, v178\n\t"
    "v_cvt_pk_bf16_f32 v195, v163, v179\n\t"
    "v_cvt_pk_bf16_f32 v196, v164, v180\n\t"
    "v_cvt_pk_bf16_f32 v197, v165, v181\n\t"
    "v_cvt_pk_bf16_f32 v198, v166, v182\n\t"
    "v_cvt_pk_bf16_f32 v199, v167, v183\n\t"
    "v_cvt_pk_bf16_f32 v200, v168, v184\n\t"
    "v_cvt_pk_bf16_f32 v201, v169, v185\n\t"
    "v_cvt_pk_bf16_f32 v202, v170, v186\n\t"
    "v_cvt_pk_bf16_f32 v203, v171, v187\n\t"
    "v_cvt_pk_bf16_f32 v204, v172, v188\n\t"
    "v_cvt_pk_bf16_f32 v205, v173, v189\n\t"
    "v_cvt_pk_bf16_f32 v206, v174, v190\n\t"
    "v_cvt_pk_bf16_f32 v207, v175, v191\n\t"
    "v_mfma_f32_16x16x32_bf16 a[196:199], a[160:163], v[0:3], 0\n\t"
    "v_accvgpr_write_b32 a16, v0\n\t"
    "v_accvgpr_write_b32 a17, v1\n\t"
    "v_accvgpr_write_b32 a18, v2\n\t"
    "v_accvgpr_write_b32 a19, v3\n\t"
    "v_mfma_f32_16x16x32_bf16 a[196:199], a[164:167], v[4:7], a[196:199]\n\t"
    "v_accvgpr_write_b32 a20, v4\n\t"
    "v_accvgpr_write_b32 a21, v5\n\t"
    "v_accvgpr_write_b32 a22, v6\n\t"
    "v_accvgpr_write_b32 a23, v7\n\t"
    "v_mfma_f32_16x16x32_bf16 a[196:199], a[168:171], v[8:11], a[196:199]\n\t"
    "v_accvgpr_write_b32 a24, v8\n\t"
    "v_accvgpr_write_b32 a25, v9\n\t"
    "v_accvgpr_write_b32 a26, v10\n\t"
    "v_accvgpr_write_b32 a27, v11\n\t"
    "v_mfma_f32_16x16x32_bf16 a[196:199], a[172:175], v[12:15], a[196:199]\n\t"
    "v_accvgpr_write_b32 a28, v12\n\t"
    "v_accvgpr_write_b32 a29, v13\n\t"
    "v_accvgpr_write_b32 a30, v14\n\t"
    "v_accvgpr_write_b32 a31, v15\n\t"
    "v_mfma_f32_16x16x32_bf16 a[196:199], a[176:179], v[192:195], a[196:199]\n\t"
    "v_accvgpr_write_b32 a80, v192\n\t"
    "v_accvgpr_write_b32 a81, v193\n\t"
    "v_accvgpr_write_b32 a82, v194\n\t"
    "v_accvgpr_write_b32 a83, v195\n\t"
    "v_mfma_f32_16x16x32_bf16 a[196:199], a[180:183], v[196:199], a[196:199]\n\t"
    "v_accvgpr_write_b32 a84, v196\n\t"
    "v_accvgpr_write_b32 a85, v197\n\t"
    "v_accvgpr_write_b32 a86, v198\n\t"
    "v_accvgpr_write_b32 a87, v199\n\t"
    "v_mfma_f32_16x16x32_bf16 a[196:199], a[184:187], v[200:203], a[196:199]\n\t"
    "v_accvgpr_write_b32 a88, v200\n\t"
    "v_accvgpr_write_b32 a89, v201\n\t"
    "v_accvgpr_write_b32 a90, v202\n\t"
    "v_accvgpr_write_b32 a91, v203\n\t"
    "v_mfma_f32_16x16x32_bf16 a[196:199], a[188:191], v[204:207], a[196:199]\n\t"
    "v_accvgpr_write_b32 a92, v204\n\t"
    "v_accvgpr_write_b32 a93, v205\n\t"
    "v_accvgpr_write_b32 a94, v206\n\t"
    "v_accvgpr_write_b32 a95, v207\n\t"
    "s_cmp_lt_u32 %3, 2\n\t"
    "s_cbranch_scc0 L_sk13_%=\n\t"
    "s_waitcnt vmcnt(8)\n\t"
    "v_add_u32 v47, 1, v47\n\t"
    "ds_write_b32 v46, v47\n\t"
    "s_waitcnt vmcnt(4)\n\t"
    "L_sk13_%=:\n\t"
    "s_cmp_lt_u32 %3, 2\n\t"
    "s_cbranch_scc1 L_sk14_%=\n\t"
    "s_waitcnt vmcnt(0)\n\t"
    "s_mov_b64 exec, 15\n\t"
    "global_store_dwordx2 v38, v[36:37], s[26:27] sc1\n\t"
    "s_mov_b64 exec, -1\n\t"
    "L_wq12_%=:\n\t"
    "ds_read_b32 v41, v46\n\t"
    "s_waitcnt lgkmcnt(0)\n\t"
    "v_readfirstlane_b32 s60, v41\n\t"
    "s_and_b32 s61, s60, 255\n\t"
    "s_cmp_le_u32 s61, 2\n\t"
    "s_cbranch_scc1 L_wq12_%=\n\t"
    "L_sk14_%=:\n\t"
    "ds_read_b128 v[208:211], v32 offset:8192\n\t"
    "ds_read_b128 v[212:215], v33 offset:8192\n\t"
    "ds_read_b128 v[216:219], v34 offset:8192\n\t"
    "ds_read_b128 v[220:223], v35 offset:8192\n\t"
    "s_waitcnt lgkmcnt(0)\n\t"
    "v_lshlrev_b32 v128, 16, v208\n\t"
    "v_and_b32 v144, 0xffff0000, v208\n\t"
    "v_lshlrev_b32 v129, 16, v209\n\t"
    "v_and_b32 v145, 0xffff0000, v209\n\t"
    "v_lshlrev_b32 v130, 16, v210\n\t"
    "v_and_b32 v146, 0xffff0000, v210\n\t"
    "v_lshlrev_b32 v131, 16, v211\n\t"
    "v_and_b32 v147, 0xffff0000, v211\n\t"
    "v_lshlrev_b32 v132, 16, v212\n\t"
    "v_and_b32 v148, 0xffff0000, v212\n\t"
    "v_lshlrev_b32 v133, 16, v213\n\t"
    "v_and_b32 v149, 0xffff0000, v213\n\t"
    "v_lshlrev_b32 v134, 16, v214\n\t"
    "v_and_b32 v150, 0xffff0000, v214\n\t"
    "v_lshlrev_b32 v135, 16, v215\n\t"
    "v_and_b32 v151, 0xffff0000, v215\n\t"
    "v_lshlrev_b32 v136, 16, v216\n\t"
    "v_and_b32 v152, 0xffff0000, v216\n\t"
    "v_lshlrev_b32 v137, 16, v217\n\t"
    "v_and_b32 v153, 0xffff0000, v217\n\t"
    "v_lshlrev_b32 v138, 16, v218\n\t"
    "v_and_b32 v154, 0xffff0000, v218\n\t"
    "v_lshlrev_b32 v139, 16, v219\n\t"
    "v_and_b32 v155, 0xffff0000, v219\n\t"
    "v_lshlrev_b32 v140, 16, v220\n\t"
    "v_and_b32 v156, 0xffff0000, v220\n\t"
    "v_lshlrev_b32 v141, 16, v221\n\t"
    "v_and_b32 v157, 0xffff0000, v221\n\t"
    "v_lshlrev_b32 v142, 16, v222\n\t"
    "v_and_b32 v158, 0xffff0000, v222\n\t"
    "v_lshlrev_b32 v143, 16, v223\n\t"
    "v_and_b32 v159, 0xffff0000, v223\n\t"
    "v_pk_mul_f32 v[160:161], v[128:129], v[64:65]\n\t"
    "v_pk_mul_f32 v[162:163], v[130:131], v[66:67]\n\t"
    "v_pk_mul_f32 v[164:165], v[132:133], v[68:69]\n\t"
    "v_pk_mul_f32 v[166:167], v[134:135], v[70:71]\n\t"
    "v_pk_mul_f32 v[168:169], v[136:137], v[72:73]\n\t"
    "v_pk_mul_f32 v[170:171], v[138:139], v[74:75]\n\t"
    "v_pk_mul_f32 v[172:173], v[140:141], v[76:77]\n\t"
    "v_pk_mul_f32 v[174:175], v[142:143], v[78:79]\n\t"
    "v_pk_mul_f32 v[176:177], v[144:145], v[80:81]\n\t"
    "v_pk_mul_f32 v[178:179], v[146:147], v[82:83]\n\t"
    "v_pk_mul_f32 v[180:181], v[148:149], v[84:85]\n\t"
    "v_pk_mul_f32 v[182:183], v[150:151], v[86:87]\n\t"
    "v_pk_mul_f32 v[184:185], v[152:153], v[88:89]\n\t"
    "v_pk_mul_f32 v[186:187], v[154:155], v[90:91]\n\t"
    "v_pk_mul_f32 v[188:189], v[156:157], v[92:93]\n\t"
    "v_pk_mul_f32 v[190:191], v[158:159], v[94:95]\n\t"
    "v_cvt_pk_bf16_f32 v0, v160, v176\n\t"
    "v_cvt_pk_bf16_f32 v1, v161, v177\n\t"
    "v_cvt_pk_bf16_f32 v2, v162, v178\n\t"
    "v_cvt_pk_bf16_f32 v3, v163, v179\n\t"
    "v_cvt_pk_bf16_f32 v4, v164, v180\n\t"
    "v_cvt_pk_bf16_f32 v5, v165, v181\n\t"
    "v_cvt_pk_bf16_f32 v6, v166, v182\n\t"
    "v_cvt_pk_bf16_f32 v7, v167, v183\n\t"
    "v_cvt_pk_bf16_f32 v8, v168, v184\n\t"
    "v_cvt_pk_bf16_f32 v9, v169, v185\n\t"
    "v_cvt_pk_bf16_f32 v10, v170, v186\n\t"
    "v_cvt_pk_bf16_f32 v11, v171, v187\n\t"
    "v_cvt_pk_bf16_f32 v12, v172, v188\n\t"
    "v_cvt_pk_bf16_f32 v13, v173, v189\n\t"
    "v_cvt_pk_bf16_f32 v14, v174, v190\n\t"
    "v_cvt_pk_bf16_f32 v15, v175, v191\n\t"
    "v_pk_mul_f32 v[160:161], v[128:129], v[96:97]\n\t"
    "v_pk_mul_f32 v[162:163], v[130:131], v[98:99]\n\t"
    "v_pk_mul_f32 v[164:165], v[132:133], v[100:101]\n\t"
    "v_pk_mul_f32 v[166:167], v[134:135], v[102:103]\n\t"
    "v_pk_mul_f32 v[168:169], v[136:137], v[104:105]\n\t"
    "v_pk_mul_f32 v[170:171], v[138:139], v[106:107]\n\t"
    "v_pk_mul_f32 v[172:173], v[140:141], v[108:109]\n\t"
    "v_pk_mul_f32 v[174:175], v[142:143], v[110:111]\n\t"
    "v_pk_mul_f32 v[176:177], v[144:145], v[112:113]\n\t"
    "v_pk_mul_f32 v[178:179], v[146:147], v[114:115]\n\t"
    "v_pk_mul_f32 v[180:181], v[148:149], v[116:117]\n\t"
    "v_pk_mul_f32 v[182:183], v[150:151], v[118:119]\n\t"
    "v_pk_mul_f32 v[184:185], v[152:153], v[120:121]\n\t"
    "v_pk_mul_f32 v[186:187], v[154:155], v[122:123]\n\t"
    "v_pk_mul_f32 v[188:189], v[156:157], v[124:125]\n\t"
    "v_pk_mul_f32 v[190:191], v[158:159], v[126:127]\n\t"
    "v_cvt_pk_bf16_f32 v192, v160, v176\n\t"
    "v_cvt_pk_bf16_f32 v193, v161, v177\n\t"
    "v_cvt_pk_bf16_f32 v194, v162, v178\n\t"
    "v_cvt_pk_bf16_f32 v195, v163, v179\n\t"
    "v_cvt_pk_bf16_f32 v196, v164, v180\n\t"
    "v_cvt_pk_bf16_f32 v197, v165, v181\n\t"
    "v_cvt_pk_bf16_f32 v198, v166, v182\n\t"
    "v_cvt_pk_bf16_f32 v199, v167, v183\n\t"
    "v_cvt_pk_bf16_f32 v200, v168, v184\n\t"
    "v_cvt_pk_bf16_f32 v201, v169, v185\n\t"
    "v_cvt_pk_bf16_f32 v202, v170, v186\n\t"
    "v_cvt_pk_bf16_f32 v203, v171, v187\n\t"
    "v_cvt_pk_bf16_f32 v204, v172, v188\n\t"
    "v_cvt_pk_bf16_f32 v205, v173, v189\n\t"
    "v_cvt_pk_bf16_f32 v206, v174, v190\n\t"
    "v_cvt_pk_bf16_f32 v207, v175, v191\n\t"
    "v_mfma_f32_16x16x32_bf16 a[200:203], a[160:163], v[0:3], 0\n\t"
    "v_accvgpr_write_b32 a32, v0\n\t"
    "v_accvgpr_write_b32 a33, v1\n\t"
    "v_accvgpr_write_b32 a34, v2\n\t"
    "v_accvgpr_write_b32 a35, v3\n\t"
    "v_mfma_f32_16x16x32_bf16 a[200:203], a[164:167], v[4:7], a[200:203]\n\t"
    "v_accvgpr_write_b32 a36, v4\n\t"
    "v_accvgpr_write_b32 a37, v5\n\t"
    "v_accvgpr_write_b32 a38, v6\n\t"
    "v_accvgpr_write_b32 a39, v7\n\t"
    "v_mfma_f32_16x16x32_bf16 a[200:203], a[168:171], v[8:11], a[200:203]\n\t"
    "v_accvgpr_write_b32 a40, v8\n\t"
    "v_accvgpr_write_b32 a41, v9\n\t"
    "v_accvgpr_write_b32 a42, v10\n\t"
    "v_accvgpr_write_b32 a43, v11\n\t"
    "v_mfma_f32_16x16x32_bf16 a[200:203], a[172:175], v[12:15], a[200:203]\n\t"
    "v_accvgpr_write_b32 a44, v12\n\t"
    "v_accvgpr_write_b32 a45, v13\n\t"
    "v_accvgpr_write_b32 a46, v14\n\t"
    "v_accvgpr_write_b32 a47, v15\n\t"
    "v_mfma_f32_16x16x32_bf16 a[200:203], a[176:179], v[192:195], a[200:203]\n\t"
    "v_accvgpr_write_b32 a96, v192\n\t"
    "v_accvgpr_write_b32 a97, v193\n\t"
    "v_accvgpr_write_b32 a98, v194\n\t"
    "v_accvgpr_write_b32 a99, v195\n\t"
    "v_mfma_f32_16x16x32_bf16 a[200:203], a[180:183], v[196:199], a[200:203]\n\t"
    "v_accvgpr_write_b32 a100, v196\n\t"
    "v_accvgpr_write_b32 a101, v197\n\t"
    "v_accvgpr_write_b32 a102, v198\n\t"
    "v_accvgpr_write_b32 a103, v199\n\t"
    "v_mfma_f32_16x16x32_bf16 a[200:203], a[184:187], v[200:203], a[200:203]\n\t"
    "v_accvgpr_write_b32 a104, v200\n\t"
    "v_accvgpr_write_b32 a105, v201\n\t"
    "v_accvgpr_write_b32 a106, v202\n\t"
    "v_accvgpr_write_b32 a107, v203\n\t"
    "v_mfma_f32_16x16x32_bf16 a[200:203], a[188:191], v[204:207], a[200:203]\n\t"
    "v_accvgpr_write_b32 a108, v204\n\t"
    "v_accvgpr_write_b32 a109, v205\n\t"
    "v_accvgpr_write_b32 a110, v206\n\t"
    "v_accvgpr_write_b32 a111, v207\n\t"
    "s_cmp_lt_u32 %3, 2\n\t"
    "s_cbranch_scc0 L_sk16_%=\n\t"
    "s_waitcnt vmcnt(0)\n\t"
    "L_sk16_%=:\n\t"
    "s_cmp_lt_u32 %3, 2\n\t"
    "s_cbranch_scc1 L_sk17_%=\n\t"
    "L_wq15_%=:\n\t"
    "ds_read_b32 v41, v46\n\t"
    "s_waitcnt lgkmcnt(0)\n\t"
    "v_readfirstlane_b32 s60, v41\n\t"
    "s_and_b32 s61, s60, 255\n\t"
    "s_cmp_le_u32 s61, 3\n\t"
    "s_cbranch_scc1 L_wq15_%=\n\t"
    "L_sk17_%=:\n\t"
    "ds_read_b128 v[208:211], v32 offset:12288\n\t"
    "ds_read_b128 v[212:215], v33 offset:12288\n\t"
    "ds_read_b128 v[216:219], v34 offset:12288\n\t"
    "ds_read_b128 v[220:223], v35 offset:12288\n\t"
    "s_waitcnt lgkmcnt(0)\n\t"
    "v_lshlrev_b32 v128, 16, v208\n\t"
    "v_and_b32 v144, 0xffff0000, v208\n\t"
    "v_lshlrev_b32 v129, 16, v209\n\t"
    "v_and_b32 v145, 0xffff0000, v209\n\t"
    "v_lshlrev_b32 v130, 16, v210\n\t"
    "v_and_b32 v146, 0xffff0000, v210\n\t"
    "v_lshlrev_b32 v131, 16, v211\n\t"
    "v_and_b32 v147, 0xffff0000, v211\n\t"
    "v_lshlrev_b32 v132, 16, v212\n\t"
    "v_and_b32 v148, 0xffff0000, v212\n\t"
    "v_lshlrev_b32 v133, 16, v213\n\t"
    "v_and_b32 v149, 0xffff0000, v213\n\t"
    "v_lshlrev_b32 v134, 16, v214\n\t"
    "v_and_b32 v150, 0xffff0000, v214\n\t"
    "v_lshlrev_b32 v135, 16, v215\n\t"
    "v_and_b32 v151, 0xffff0000, v215\n\t"
    "v_lshlrev_b32 v136, 16, v216\n\t"
    "v_and_b32 v152, 0xffff0000, v216\n\t"
    "v_lshlrev_b32 v137, 16, v217\n\t"
    "v_and_b32 v153, 0xffff0000, v217\n\t"
    "v_lshlrev_b32 v138, 16, v218\n\t"
    "v_and_b32 v154, 0xffff0000, v218\n\t"
    "v_lshlrev_b32 v139, 16, v219\n\t"
    "v_and_b32 v155, 0xffff0000, v219\n\t"
    "v_lshlrev_b32 v140, 16, v220\n\t"
    "v_and_b32 v156, 0xffff0000, v220\n\t"
    "v_lshlrev_b32 v141, 16, v221\n\t"
    "v_and_b32 v157, 0xffff0000, v221\n\t"
    "v_lshlrev_b32 v142, 16, v222\n\t"
    "v_and_b32 v158, 0xffff0000, v222\n\t"
    "v_lshlrev_b32 v143, 16, v223\n\t"
    "v_and_b32 v159, 0xffff0000, v223\n\t"
    "v_pk_mul_f32 v[160:161], v[128:129], v[64:65]\n\t"
    "v_pk_mul_f32 v[162:163], v[130:131], v[66:67]\n\t"
    "v_pk_mul_f32 v[164:165], v[132:133], v[68:69]\n\t"
    "v_pk_mul_f32 v[166:167], v[134:135], v[70:71]\n\t"
    "v_pk_mul_f32 v[168:169], v[136:137], v[72:73]\n\t"
    "v_pk_mul_f32 v[170:171], v[138:139], v[74:75]\n\t"
    "v_pk_mul_f32 v[172:173], v[140:141], v[76:77]\n\t"
    "v_pk_mul_f32 v[174:175], v[142:143], v[78:79]\n\t"
    "v_pk_mul_f32 v[176:177], v[144:145], v[80:81]\n\t"
    "v_pk_mul_f32 v[178:179], v[146:147], v[82:83]\n\t"
    "v_pk_mul_f32 v[180:181], v[148:149], v[84:85]\n\t"
    "v_pk_mul_f32 v[182:183], v[150:151], v[86:87]\n\t"
    "v_pk_mul_f32 v[184:185], v[152:153], v[88:89]\n\t"
    "v_pk_mul_f32 v[186:187], v[154:155], v[90:91]\n\t"
    "v_pk_mul_f32 v[188:189], v[156:157], v[92:93]\n\t"
    "v_pk_mul_f32 v[190:191], v[158:159], v[94:95]\n\t"
    "v_cvt_pk_bf16_f32 v0, v160, v176\n\t"
    "v_cvt_pk_bf16_f32 v1, v161, v177\n\t"
    "v_cvt_pk_bf16_f32 v2, v162, v178\n\t"
    "v_cvt_pk_bf16_f32 v3, v163, v179\n\t"
    "v_cvt_pk_bf16_f32 v4, v164, v180\n\t"
    "v_cvt_pk_bf16_f32 v5, v165, v181\n\t"
    "v_cvt_pk_bf16_f32 v6, v166, v182\n\t"
    "v_cvt_pk_bf16_f32 v7, v167, v183\n\t"
    "v_cvt_pk_bf16_f32 v8, v168, v184\n\t"
    "v_cvt_pk_bf16_f32 v9, v169, v185\n\t"
    "v_cvt_pk_bf16_f32 v10, v170, v186\n\t"
    "v_cvt_pk_bf16_f32 v11, v171, v187\n\t"
    "v_cvt_pk_bf16_f32 v12, v172, v188\n\t"
    "v_cvt_pk_bf16_f32 v13, v173, v189\n\t"
    "v_cvt_pk_bf16_f32 v14, v174, v190\n\t"
    "v_cvt_pk_bf16_f32 v15, v175, v191\n\t"
    "v_pk_mul_f32 v[160:161], v[128:129], v[96:97]\n\t"
    "v_pk_mul_f32 v[162:163], v[130:131], v[98:99]\n\t"
    "v_pk_mul_f32 v[164:165], v[132:133], v[100:101]\n\t"
    "v_pk_mul_f32 v[166:167], v[134:135], v[102:103]\n\t"
    "v_pk_mul_f32 v[168:169], v[136:137], v[104:105]\n\t"
    "v_pk_mul_f32 v[170:171], v[138:139], v[106:107]\n\t"
    "v_pk_mul_f32 v[172:173], v[140:141], v[108:109]\n\t"
    "v_pk_mul_f32 v[174:175], v[142:143], v[110:111]\n\t"
    "v_pk_mul_f32 v[176:177], v[144:145], v[112:113]\n\t"
    "v_pk_mul_f32 v[178:179], v[146:147], v[114:115]\n\t"
    "v_pk_mul_f32 v[180:181], v[148:149], v[116:117]\n\t"
    "v_pk_mul_f32 v[182:183], v[150:151], v[118:119]\n\t"
    "v_pk_mul_f32 v[184:185], v[152:153], v[120:121]\n\t"
    "v_pk_mul_f32 v[186:187], v[154:155], v[122:123]\n\t"
    "v_pk_mul_f32 v[188:189], v[156:157], v[124:125]\n\t"
    "v_pk_mul_f32 v[190:191], v[158:159], v[126:127]\n\t"
    "v_cvt_pk_bf16_f32 v192, v160, v176\n\t"
    "v_cvt_pk_bf16_f32 v193, v161, v177\n\t"
    "v_cvt_pk_bf16_f32 v194, v162, v178\n\t"
    "v_cvt_pk_bf16_f32 v195, v163, v179\n\t"
    "v_cvt_pk_bf16_f32 v196, v164, v180\n\t"
    "v_cvt_pk_bf16_f32 v197, v165, v181\n\t"
    "v_cvt_pk_bf16_f32 v198, v166, v182\n\t"
    "v_cvt_pk_bf16_f32 v199, v167, v183\n\t"
    "v_cvt_pk_bf16_f32 v200, v168, v184\n\t"
    "v_cvt_pk_bf16_f32 v201, v169, v185\n\t"
    "v_cvt_pk_bf16_f32 v202, v170, v186\n\t"
    "v_cvt_pk_bf16_f32 v203, v171, v187\n\t"
    "v_cvt_pk_bf16_f32 v204, v172, v188\n\t"
    "v_cvt_pk_bf16_f32 v205, v173, v189\n\t"
    "v_cvt_pk_bf16_f32 v206, v174, v190\n\t"
    "v_cvt_pk_bf16_f32 v207, v175, v191\n\t"
    "v_mfma_f32_16x16x32_bf16 a[204:207], a[160:163], v[0:3], 0\n\t"
    "v_accvgpr_write_b32 a48, v0\n\t"
    "v_accvgpr_write_b32 a49, v1\n\t"
    "v_accvgpr_write_b32 a50, v2\n\t"
    "v_accvgpr_write_b32 a51, v3\n\t"
    "v_mfma_f32_16x16x32_bf16 a[204:207], a[164:167], v[4:7], a[204:207]\n\t"
    "v_accvgpr_write_b32 a52, v4\n\t"
    "v_accvgpr_write_b32 a53, v5\n\t"
    "v_accvgpr_write_b32 a54, v6\n\t"
    "v_accvgpr_write_b32 a55, v7\n\t"
    "v_mfma_f32_16x16x32_bf16 a[204:207], a[168:171], v[8:11], a[204:207]\n\t"
    "v_accvgpr_write_b32 a56, v8\n\t"
    "v_accvgpr_write_b32 a57, v9\n\t"
    "v_accvgpr_write_b32 a58, v10\n\t"
    "v_accvgpr_write_b32 a59, v11\n\t"
    "v_mfma_f32_16x16x32_bf16 a[204:207], a[172:175], v[12:15], a[204:207]\n\t"
    "v_accvgpr_write_b32 a60, v12\n\t"
    "v_accvgpr_write_b32 a61, v13\n\t"
    "v_accvgpr_write_b32 a62, v14\n\t"
    "v_accvgpr_write_b32 a63, v15\n\t"
    "v_mfma_f32_16x16x32_bf16 a[204:207], a[176:179], v[192:195], a[204:207]\n\t"
    "v_accvgpr_write_b32 a112, v192\n\t"
    "v_accvgpr_write_b32 a113, v193\n\t"
    "v_accvgpr_write_b32 a114, v194\n\t"
    "v_accvgpr_write_b32 a115, v195\n\t"
    "v_mfma_f32_16x16x32_bf16 a[204:207], a[180:183], v[196:199], a[204:207]\n\t"
    "v_accvgpr_write_b32 a116, v196\n\t"
    "v_accvgpr_write_b32 a117, v197\n\t"
    "v_accvgpr_write_b32 a118, v198\n\t"
    "v_accvgpr_write_b32 a119, v199\n\t"
    "v_mfma_f32_16x16x32_bf16 a[204:207], a[184:187], v[200:203], a[204:207]\n\t"
    "v_accvgpr_write_b32 a120, v200\n\t"
    "v_accvgpr_write_b32 a121, v201\n\t"
    "v_accvgpr_write_b32 a122, v202\n\t"
    "v_accvgpr_write_b32 a123, v203\n\t"
    "v_mfma_f32_16x16x32_bf16 a[204:207], a[188:191], v[204:207], a[204:207]\n\t"
    "v_accvgpr_write_b32 a124, v204\n\t"
    "v_accvgpr_write_b32 a125, v205\n\t"
    "v_accvgpr_write_b32 a126, v206\n\t"
    "v_accvgpr_write_b32 a127, v207\n\t"
    "v_mfma_f32_32x32x16_bf16 v[0:15], a[208:211], a[208:211], 0\n\t"
    "v_mfma_f32_32x32x16_bf16 v[16:31], a[208:211], a[208:211], 0\n\t"
    "v_mfma_f32_32x32x16_bf16 v[32:47], a[208:211], a[208:211], 0\n\t"
    "v_mfma_f32_32x32x16_bf16 v[48:63], a[208:211], a[208:211], 0\n\t"
    "v_mfma_f32_32x32x16_bf16 v[64:79], a[208:211], a[208:211], 0\n\t"
    "v_mfma_f32_32x32x16_bf16 v[80:95], a[208:211], a[208:211], 0\n\t"
    "v_mfma_f32_32x32x16_bf16 v[96:111], a[208:211], a[208:211], 0\n\t"
    "v_mfma_f32_32x32x16_bf16 v[112:127], a[208:211], a[208:211], 0\n\t"
    "v_accvgpr_mov_b32 a244, a192\n\t"
    "v_accvgpr_mov_b32 a245, a196\n\t"
    "v_accvgpr_mov_b32 a246, a200\n\t"
    "v_accvgpr_mov_b32 a247, a204\n\t"
    "s_cmp_lt_u32 %3, 2\n\t"
    "s_cbranch_scc1 L_sk21_%=\n\t"
    "s_lshl_b32 s60, s28, 1\n\t"
    "s_add_u32 s60, s60, %3\n\t"
    "s_and_b32 s60, s60, 3\n\t"
    "s_mul_i32 s60, s60, 1280\n\t"
    "v_add_u32 v248, s60, v241\n\t"
    "s_mov_b32 s54, 0\n\t"
    "L_poll18_%=:\n\t"
    "global_load_dwordx4 v[244:247], v248, s[26:27] sc1\n\t"
    "s_waitcnt vmcnt(0)\n\t"
    "v_xor_b32 v249, s66, v244\n\t"
    "v_xor_b32 v250, s66, v246\n\t"
    "v_xor_b32 v251, s67, v245\n\t"
    "v_xor_b32 v252, s67, v247\n\t"
    "v_or_b32 v251, v251, v252\n\t"
    "v_and_b32 v252, -2, v251\n\t"
    "v_or3_b32 v249, v249, v250, v252\n\t"
    "v_cmp_ne_u32 vcc, 0, v249\n\t"
    "s_cmp_eq_u64 vcc, 0\n\t"
    "s_cbranch_scc1 L_pok19_%=\n\t"
    "s_sleep 6\n\t"
    "s_add_u32 s54, s54, 1\n\t"
    "s_cmp_lt_u32 s54, 65536\n\t"
    "s_cbranch_scc1 L_poll18_%=\n\t"
    "v_mov_b32 v251, 1\n\t"
    "s_branch L_pout20_%=\n\t"
    "L_pok19_%=:\n\t"
    "s_and_b32 s60, s28, 15\n\t"
    "s_cmp_eq_u32 %3, 2\n\t"
    "s_cselect_b32 s64, s60, -1\n\t"
    "L_pout20_%=:\n\t"
    "s_lshl_b32 s61, s55, 12\n\t"
    "s_add_u32 s60, s61, 1032192\n\t"
    "s_add_u32 s61, s61, 133120\n\t"
    "s_add_u32 s65, s60, 0\n\t"
    "s_add_u32 m0, s61, 0\n\t"
    "s_nop 0\n\t"
    "buffer_load_dwordx4 v241, s[40:43], s65 offen offset:0 sc1 lds\n\t"
    "buffer_load_dwordx4 v241, s[40:43], s65 offen offset:1024 sc1 lds\n\t"
    "buffer_load_dwordx4 v241, s[40:43], s65 offen offset:2048 sc1 lds\n\t"
    "buffer_load_dwordx4 v241, s[40:43], s65 offen offset:3072 sc1 lds\n\t"
    "s_lshl_b32 s60, s55, 13\n\t"
    "s_add_u32 s65, s60, 0\n\t"
    "s_add_u32 m0, s60, 0\n\t"
    "s_nop 0\n\t"
    "buffer_load_dwordx4 v241, s[40:43], s65 offen offset:0 sc1 lds\n\t"
    "buffer_load_dwordx4 v241, s[40:43], s65 offen offset:1024 sc1 lds\n\t"
    "buffer_load_dwordx4 v241, s[40:43], s65 offen offset:2048 sc1 lds\n\t"
    "buffer_load_dwordx4 v241, s[40:43], s65 offen offset:3072 sc1 lds\n\t"
    "s_add_u32 s65, s60, 4096\n\t"
    "s_add_u32 m0, s60, 4096\n\t"
    "s_nop 0\n\t"
    "buffer_load_dwordx4 v241, s[40:43], s65 offen offset:0 sc1 lds\n\t"
    "buffer_load_dwordx4 v241, s[40:43], s65 offen offset:1024 sc1 lds\n\t"
    "buffer_load_dwordx4 v241, s[40:43], s65 offen offset:2048 sc1 lds\n\t"
    "buffer_load_dwordx4 v241, s[40:43], s65 offen offset:3072 sc1 lds\n\t"
    "s_mul_i32 s60, s55, 12288\n\t"
    "s_add_u32 s60, s60, 16384\n\t"
    "s_add_u32 s65, s60, 0\n\t"
    "s_add_u32 m0, s60, 0\n\t"
    "s_nop 0\n\t"
    "buffer_load_dwordx4 v241, s[40:43], s65 offen offset:0 sc1 lds\n\t"
    "buffer_load_dwordx4 v241, s[40:43], s65 offen offset:1024 sc1 lds\n\t"
    "buffer_load_dwordx4 v241, s[40:43], s65 offen offset:2048 sc1 lds\n\t"
    "buffer_load_dwordx4 v241, s[40:43], s65 offen offset:3072 sc1 lds\n\t"
    "s_add_u32 s65, s60, 4096\n\t"
    "s_add_u32 m0, s60, 4096\n\t"
    "s_nop 0\n\t"
    "buffer_load_dwordx4 v241, s[40:43], s65 offen offset:0 sc1 lds\n\t"
    "buffer_load_dwordx4 v241, s[40:43], s65 offen offset:1024 sc1 lds\n\t"
    "buffer_load_dwordx4 v241, s[40:43], s65 offen offset:2048 sc1 lds\n\t"
    "buffer_load_dwordx4 v241, s[40:43], s65 offen offset:3072 sc1 lds\n\t"
    "s_add_u32 s65, s60, 8192\n\t"
    "s_add_u32 m0, s60, 8192\n\t"
    "s_nop 0\n\t"
    "buffer_load_dwordx4 v241, s[40:43], s65 offen offset:0 sc1 lds\n\t"
    "buffer_load_dwordx4 v241, s[40:43], s65 offen offset:1024 sc1 lds\n\t"
    "buffer_load_dwordx4 v241, s[40:43], s65 offen offset:2048 sc1 lds\n\t"
    "buffer_load_dwordx4 v241, s[40:43], s65 offen offset:3072 sc1 lds\n\t"
    "v_and_b32 v251, 1, v251\n\t"
    "v_cmp_ne_u32 vcc, 0, v251\n\t"
    "s_cmp_lg_u64 vcc, 0\n\t"
    "s_cselect_b32 s60, 1, 0\n\t"
    "s_lshl_b32 s61, s55, 2\n\t"
    "s_add_u32 s61, s61, 132624\n\t"
    "v_mov_b32 v249, s61\n\t"
    "v_mov_b32 v250, s60\n\t"
    "ds_write_b32 v249, v250\n\t"
    "s_waitcnt vmcnt(12) lgkmcnt(0)\n\t"
    "L_sk21_%=:\n\t"
    "v_add_u32 v237, 133120, v241\n\t"
    "v_mov_b32 v240, v241\n\t"
    "s_barrier\n\t"
    "ds_read_b128 a[128:131], v237 offset:0\n\t"
    "ds_read_b128 a[132:135], v237 offset:1024\n\t"
    "ds_read_b128 a[136:139], v237 offset:2048\n\t"
    "ds_read_b128 a[140:143], v237 offset:3072\n\t"
    "ds_read_b128 a[144:147], v237 offset:4096\n\t"
    "ds_read_b128 a[148:151], v237 offset:5120\n\t"
    "ds_read_b128 a[152:155], v237 offset:6144\n\t"
    "ds_read_b128 a[156:159], v237 offset:7168\n\t"
    "s_mul_i32 s60, %3, 6144\n\t"
    "s_add_u32 s60, s60, 40960\n\t"
    "s_waitcnt lgkmcnt(4)\n\t"
    "v_mfma_f32_16x16x32_bf16 v[128:131], a[128:131], a[0:3], 0\n\t"
    "s_add_u32 s65, s60, 0\n\t"
    "s_add_u32 m0, s60, 0\n\t"
    "s_nop 0\n\t"
    "buffer_load_dwordx4 v241, s[40:43], s65 offen offset:0 sc1 lds\n\t"
    "v_mfma_f32_16x16x32_bf16 v[136:139], a[128:131], a[16:19], 0\n\t"
    "ds_read_b128 a[160:163], v240 offset:8192\n\t"
    "v_mfma_f32_16x16x32_bf16 v[144:147], a[128:131], a[32:35], 0\n\t"
    "buffer_load_dwordx4 v241, s[40:43], s65 offen offset:1024 sc1 lds\n\t"
    "v_mfma_f32_16x16x32_bf16 v[152:155], a[128:131], a[48:51], 0\n\t"
    "ds_read_b128 a[128:131], v240 offset:0\n\t"
    "v_mfma_f32_16x16x32_bf16 v[128:131], a[132:135], a[4:7], v[128:131]\n\t"
    "buffer_load_dwordx4 v241, s[40:43], s65 offen offset:2048 sc1 lds\n\t"
    "v_mfma_f32_16x16x32_bf16 v[136:139], a[132:135], a[20:23], v[136:139]\n\t"
    "ds_read_b128 a[164:167], v240 offset:9216\n\t"
    "v_mfma_f32_16x16x32_bf16 v[144:147], a[132:135], a[36:39], v[144:147]\n\t"
    "buffer_load_dwordx4 v241, s[40:43], s65 offen offset:3072 sc1 lds\n\t"
    "v_mfma_f32_16x16x32_bf16 v[152:155], a[132:135], a[52:55], v[152:155]\n\t"
    "ds_read_b128 a[132:135], v240 offset:1024\n\t"
    "v_mfma_f32_16x16x32_bf16 v[128:131], a[136:139], a[8:11], v[128:131]\n\t"
    "s_add_u32 s65, s60, 4096\n\t"
    "s_add_u32 m0, s60, 4096\n\t"
    "s_nop 0\n\t"
    "buffer_load_dwordx4 v241, s[40:43], s65 offen offset:0 sc1 lds\n\t"
    "v_mfma_f32_16x16x32_bf16 v[136:139], a[136:139], a[24:27], v[136:139]\n\t"
    "ds_read_b128 a[168:171], v240 offset:10240\n\t"
    "v_mfma_f32_16x16x32_bf16 v[144:147], a[136:139], a[40:43], v[144:147]\n\t"
    "buffer_load_dwordx4 v241, s[40:43], s65 offen offset:1024 sc1 lds\n\t"
    "v_mfma_f32_16x16x32_bf16 v[152:155], a[136:139], a[56:59], v[152:155]\n\t"
    "ds_read_b128 a[136:139], v240 offset:2048\n\t"
    "v_mfma_f32_16x16x32_bf16 v[128:131], a[140:143], a[12:15], v[128:131]\n\t"
    "s_add_u32 s65, s56, 0\n\t"
    "s_add_u32 m0, s57, 0\n\t"
    "s_nop 0\n\t"
    "buffer_load_dwordx4 v241, s[40:43], s65 offen offset:0 sc1 lds\n\t"
    "v_mfma_f32_16x16x32_bf16 v[136:139], a[140:143], a[28:31], v[136:139]\n\t"
    "ds_read_b128 a[172:175], v240 offset:11264\n\t"
    "v_mfma_f32_16x16x32_bf16 v[144:147], a[140:143], a[44:47], v[144:147]\n\t"
    "buffer_load_dwordx4 v241, s[40:43], s65 offen offset:1024 sc1 lds\n\t"
    "v_mfma_f32_16x16x32_bf16 v[152:155], a[140:143], a[60:63], v[152:155]\n\t"
    "ds_read_b128 a[140:143], v240 offset:3072\n\t"
    "s_waitcnt lgkmcnt(8)\n\t"
    "v_mfma_f32_16x16x32_bf16 v[132:135], a[144:147], a[0:3], 0\n\t"
    "buffer_load_dwordx4 v241, s[40:43], s65 offen offset:2048 sc1 lds\n\t"
    "v_mfma_f32_16x16x32_bf16 v[140:143], a[144:147], a[16:19], 0\n\t"
    "ds_read_b128 a[176:179], v240 offset:12288\n\t"
    "v_mfma_f32_16x16x32_bf16 v[148:151], a[144:147], a[32:35], 0\n\t"
    "buffer_load_dwordx4 v241, s[40:43], s65 offen offset:3072 sc1 lds\n\t"
    "v_mfma_f32_16x16x32_bf16 v[156:159], a[144:147], a[48:51], 0\n\t"
    "ds_read_b128 a[144:147], v240 offset:4096\n\t"
    "v_mfma_f32_16x16x32_bf16 v[132:135], a[148:151], a[4:7], v[132:135]\n\t"
    "s_add_u32 s65, s56, 4096\n\t"
    "s_add_u32 m0, s57, 4096\n\t"
    "s_nop 0\n\t"
    "buffer_load_dwordx4 v241, s[40:43], s65 offen offset:0 sc1 lds\n\t"
    "v_max3_f32 v208, v128, v129, v130\n\t"
    "v_max_f32 v208, v208, v131\n\t"
    "v_mfma_f32_16x16x32_bf16 v[140:143], a[148:151], a[20:23], v[140:143]\n\t"
    "ds_read_b128 a[180:183], v240 offset:13312\n\t"
    "v_max3_f32 v210, v136, v137, v138\n\t"
    "v_mfma_f32_16x16x32_bf16 v[148:151], a[148:151], a[36:39], v[148:151]\n\t"
    "buffer_load_dwordx4 v241, s[40:43], s65 offen offset:1024 sc1 lds\n\t"
    "v_max_f32 v210, v210, v139\n\t"
    "v_max3_f32 v212, v144, v145, v146\n\t"
    "v_mfma_f32_16x16x32_bf16 v[156:159], a[148:151], a[52:55], v[156:159]\n\t"
    "ds_read_b128 a[148:151], v240 offset:5120\n\t"
    "v_max_f32 v212, v212, v147\n\t"
    "v_mfma_f32_16x16x32_bf16 v[132:135], a[152:155], a[8:11], v[132:135]\n\t"
    "buffer_load_dwordx4 v241, s[40:43], s65 offen offset:2048 sc1 lds\n\t"
    "v_max3_f32 v214, v152, v153, v154\n\t"
    "v_max_f32 v214, v214, v155\n\t"
    "v_mfma_f32_16x16x32_bf16 v[140:143], a[152:155], a[24:27], v[140:143]\n\t"
    "ds_read_b128 a[184:187], v240 offset:14336\n\t"
    "v_mov_b32 v209, v208\n\t"
    "v_mfma_f32_16x16x32_bf16 v[148:151], a[152:155], a[40:43], v[148:151]\n\t"
    "buffer_load_dwordx4 v241, s[40:43], s65 offen offset:3072 sc1 lds\n\t"
    "v_mov_b32 v211, v210\n\t"
    "v_mov_b32 v213, v212\n\t"
    "v_mfma_f32_16x16x32_bf16 v[156:159], a[152:155], a[56:59], v[156:159]\n\t"
    "ds_read_b128 a[152:155], v240 offset:6144\n\t"
    "v_mov_b32 v215, v214\n\t"
    "v_mfma_f32_16x16x32_bf16 v[132:135], a[156:159], a[12:15], v[132:135]\n\t"
    "v_permlane32_swap_b32 v208, v209\n\t"
    "v_permlane32_swap_b32 v210, v211\n\t"
    "v_mfma_f32_16x16x32_bf16 v[140:143], a[156:159], a[28:31], v[140:143]\n\t"
    "ds_read_b128 a[188:191], v240 offset:15360\n\t"
    "v_permlane32_swap_b32 v212, v213\n\t"
    "v_mfma_f32_16x16x32_bf16 v[148:151], a[156:159], a[44:47], v[148:151]\n\t"
    "v_permlane32_swap_b32 v214, v215\n\t"
    "v_max_f32 v208, v208, v209\n\t"
    "v_mfma_f32_16x16x32_bf16 v[156:159], a[156:159], a[60:63], v[156:159]\n\t"
    "ds_read_b128 a[156:159], v240 offset:7168\n\t"
    "v_max_f32 v210, v210, v211\n\t"
    "v_max_f32 v212, v212, v213\n\t"
    "v_max_f32 v214, v214, v215\n\t"
    "v_mov_b32 v209, v208\n\t"
    "v_mov_b32 v211, v210\n\t"
    "v_mov_b32 v213, v212\n\t"
    "v_mov_b32 v215, v214\n\t"
    "v_permlane16_swap_b32 v208, v209\n\t"
    "v_permlane16_swap_b32 v210, v211\n\t"
    "v_permlane16_swap_b32 v212, v213\n\t"
    "v_permlane16_swap_b32 v214, v215\n\t"
    "v_max_f32 v208, v208, v209\n\t"
    "v_max_f32 v210, v210, v211\n\t"
    "v_max_f32 v212, v212, v213\n\t"
    "v_max_f32 v214, v214, v215\n\t"
    "v_subrev_f32 v208, 0x41400000, v208\n\t"
    "v_subrev_f32 v210, 0x41400000, v210\n\t"
    "v_subrev_f32 v212, 0x41400000, v212\n\t"
    "v_subrev_f32 v214, 0x41400000, v214\n\t"
    "v_sub_f32 v224, 0, v208\n\t"
    "v_sub_f32 v225, 0, v208\n\t"
    "v_sub_f32 v226, 0, v208\n\t"
    "v_sub_f32 v227, 0, v208\n\t"
    "v_sub_f32 v228, 0, v210\n\t"
    "v_sub_f32 v229, 0, v210\n\t"
    "v_sub_f32 v230, 0, v210\n\t"
    "v_sub_f32 v231, 0, v210\n\t"
    "v_sub_f32 v232, 0, v212\n\t"
    "v_sub_f32 v233, 0, v212\n\t"
    "v_sub_f32 v234, 0, v212\n\t"
    "v_sub_f32 v235, 0, v212\n\t"
    "v_sub_f32 v236, 0, v214\n\t"
    "v_sub_f32 v237, 0, v214\n\t"
    "v_sub_f32 v238, 0, v214\n\t"
    "v_sub_f32 v239, 0, v214\n\t"
    "v_pk_add_f32 v[128:129], v[128:129], v[208:209] op_sel_hi:[1,0] neg_lo:[0,1] neg_hi:[0,1]\n\t"
    "v_pk_add_f32 v[130:131], v[130:131], v[208:209] op_sel_hi:[1,0] neg_lo:[0,1] neg_hi:[0,1]\n\t"
    "v_pk_add_f32 v[132:133], v[132:133], v[208:209] op_sel_hi:[1,0] neg_lo:[0,1] neg_hi:[0,1]\n\t"
    "v_pk_add_f32 v[134:135], v[134:135], v[208:209] op_sel_hi:[1,0] neg_lo:[0,1] neg_hi:[0,1]\n\t"
    "v_pk_add_f32 v[136:137], v[136:137], v[210:211] op_sel_hi:[1,0] neg_lo:[0,1] neg_hi:[0,1]\n\t"
    "v_pk_add_f32 v[138:139], v[138:139], v[210:211] op_sel_hi:[1,0] neg_lo:[0,1] neg_hi:[0,1]\n\t"
    "v_pk_add_f32 v[140:141], v[140:141], v[210:211] op_sel_hi:[1,0] neg_lo:[0,1] neg_hi:[0,1]\n\t"
    "v_pk_add_f32 v[142:143], v[142:143], v[210:211] op_sel_hi:[1,0] neg_lo:[0,1] neg_hi:[0,1]\n\t"
    "v_pk_add_f32 v[144:145], v[144:145], v[212:213] op_sel_hi:[1,0] neg_lo:[0,1] neg_hi:[0,1]\n\t"
    "v_pk_add_f32 v[146:147], v[146:147], v[212:213] op_sel_hi:[1,0] neg_lo:[0,1] neg_hi:[0,1]\n\t"
    "v_pk_add_f32 v[148:149], v[148:149], v[212:213] op_sel_hi:[1,0] neg_lo:[0,1] neg_hi:[0,1]\n\t"
    "v_pk_add_f32 v[150:151], v[150:151], v[212:213] op_sel_hi:[1,0] neg_lo:[0,1] neg_hi:[0,1]\n\t"
    "v_pk_add_f32 v[152:153], v[152:153], v[214:215] op_sel_hi:[1,0] neg_lo:[0,1] neg_hi:[0,1]\n\t"
    "v_pk_add_f32 v[154:155], v[154:155], v[214:215] op_sel_hi:[1,0] neg_lo:[0,1] neg_hi:[0,1]\n\t"
    "v_pk_add_f32 v[156:157], v[156:157], v[214:215] op_sel_hi:[1,0] neg_lo:[0,1] neg_hi:[0,1]\n\t"
    "v_pk_add_f32 v[158:159], v[158:159], v[214:215] op_sel_hi:[1,0] neg_lo:[0,1] neg_hi:[0,1]\n\t"
    "s_waitcnt vmcnt(8) lgkmcnt(0)\n\t"
    "s_barrier\n\t"
    "v_mfma_f32_16x16x32_bf16 v[160:163], a[128:131], a[0:3], v[224:227]\n\t"
    "v_exp_f32 v128, v128\n\t"
    "v_mfma_f32_16x16x32_bf16 v[160:163], a[132:135], a[4:7], v[160:163]\n\t"
    "ds_read_b128 a[192:195], v240 offset:24576\n\t"
    "v_exp_f32 v129, v129\n\t"
    "v_mfma_f32_16x16x32_bf16 v[160:163], a[136:139], a[8:11], v[160:163]\n\t"
    "v_exp_f32 v130, v130\n\t"
    "v_mfma_f32_16x16x32_bf16 v[160:163], a[140:143], a[12:15], v[160:163]\n\t"
    "v_exp_f32 v131, v131\n\t"
    "v_mfma_f32_16x16x32_bf16 v[168:171], a[140:143], a[28:31], v[228:231]\n\t"
    "ds_read_b128 a[196:199], v240 offset:25600\n\t"
    "v_exp_f32 v136, v136\n\t"
    "v_mfma_f32_16x16x32_bf16 v[168:171], a[136:139], a[24:27], v[168:171]\n\t"
    "v_exp_f32 v137, v137\n\t"
    "v_mfma_f32_16x16x32_bf16 v[168:171], a[132:135], a[20:23], v[168:171]\n\t"
    "v_exp_f32 v138, v138\n\t"
    "v_mfma_f32_16x16x32_bf16 v[168:171], a[128:131], a[16:19], v[168:171]\n\t"
    "ds_read_b128 a[200:203], v240 offset:26624\n\t"
    "v_exp_f32 v139, v139\n\t"
    "v_mfma_f32_16x16x32_bf16 v[176:179], a[128:131], a[32:35], v[232:235]\n\t"
    "v_exp_f32 v144, v144\n\t"
    "v_mfma_f32_16x16x32_bf16 v[176:179], a[132:135], a[36:39], v[176:179]\n\t"
    "v_exp_f32 v145, v145\n\t"
    "v_mfma_f32_16x16x32_bf16 v[176:179], a[136:139], a[40:43], v[176:179]\n\t"
    "ds_read_b128 a[204:207], v240 offset:27648\n\t"
    "v_exp_f32 v146, v146\n\t"
    "v_mfma_f32_16x16x32_bf16 v[176:179], a[140:143], a[44:47], v[176:179]\n\t"
    "v_exp_f32 v147, v147\n\t"
    "v_mfma_f32_16x16x32_bf16 v[184:187], a[140:143], a[60:63], v[236:239]\n\t"
    "v_exp_f32 v152, v152\n\t"
    "v_mfma_f32_16x16x32_bf16 v[184:187], a[136:139], a[56:59], v[184:187]\n\t"
    "ds_read_b128 a[140:143], v240 offset:19456\n\t"
    "v_exp_f32 v153, v153\n\t"
    "v_mfma_f32_16x16x32_bf16 v[184:187], a[132:135], a[52:55], v[184:187]\n\t"
    "v_exp_f32 v154, v154\n\t"
    "v_mfma_f32_16x16x32_bf16 v[184:187], a[128:131], a[48:51], v[184:187]\n\t"
    "v_exp_f32 v155, v155\n\t"
    "v_mfma_f32_16x16x32_bf16 v[164:167], a[144:147], a[0:3], v[224:227]\n\t"
    "ds_read_b128 a[128:131], v240 offset:16384\n\t"
    "v_exp_f32 v132, v132\n\t"
    "v_mfma_f32_16x16x32_bf16 v[164:167], a[148:151], a[4:7], v[164:167]\n\t"
    "v_exp_f32 v133, v133\n\t"
    "v_mfma_f32_16x16x32_bf16 v[164:167], a[152:155], a[8:11], v[164:167]\n\t"
    "v_exp_f32 v134, v134\n\t"
    "v_mfma_f32_16x16x32_bf16 v[164:167], a[156:159], a[12:15], v[164:167]\n\t"
    "ds_read_b128 a[132:135], v240 offset:17408\n\t"
    "v_exp_f32 v135, v135\n\t"
    "v_mfma_f32_16x16x32_bf16 v[172:175], a[156:159], a[28:31], v[228:231]\n\t"
    "v_exp_f32 v140, v140\n\t"
    "v_mfma_f32_16x16x32_bf16 v[172:175], a[152:155], a[24:27], v[172:175]\n\t"
    "v_exp_f32 v141, v141\n\t"
    "v_mfma_f32_16x16x32_bf16 v[172:175], a[148:151], a[20:23], v[172:175]\n\t"
    "ds_read_b128 a[136:139], v240 offset:18432\n\t"
    "v_exp_f32 v142, v142\n\t"
    "v_mfma_f32_16x16x32_bf16 v[172:175], a[144:147], a[16:19], v[172:175]\n\t"
    "v_exp_f32 v143, v143\n\t"
    "v_mfma_f32_16x16x32_bf16 v[180:183], a[144:147], a[32:35], v[232:235]\n\t"
    "v_exp_f32 v148, v148\n\t"
    "v_mfma_f32_16x16x32_bf16 v[180:183], a[148:151], a[36:39], v[180:183]\n\t"
    "v_exp_f32 v149, v149\n\t"
    "v_mfma_f32_16x16x32_bf16 v[180:183], a[152:155], a[40:43], v[180:183]\n\t"
    "v_exp_f32 v150, v150\n\t"
    "v_mfma_f32_16x16x32_bf16 v[180:183], a[156:159], a[44:47], v[180:183]\n\t"
    "v_exp_f32 v151, v151\n\t"
    "v_mfma_f32_16x16x32_bf16 v[188:191], a[156:159], a[60:63], v[236:239]\n\t"
    "v_exp_f32 v156, v156\n\t"
    "v_mfma_f32_16x16x32_bf16 v[188:191], a[152:155], a[56:59], v[188:191]\n\t"
    "ds_read_b128 a[156:159], v240 offset:23552\n\t"
    "v_exp_f32 v157, v157\n\t"
    "v_mfma_f32_16x16x32_bf16 v[188:191], a[148:151], a[52:55], v[188:191]\n\t"
    "ds_read_b128 a[152:155], v240 offset:22528\n\t"
    "v_exp_f32 v158, v158\n\t"
    "v_mfma_f32_16x16x32_bf16 v[188:191], a[144:147], a[48:51], v[188:191]\n\t"
    "ds_read_b128 a[148:151], v240 offset:21504\n\t"
    "v_exp_f32 v159, v159\n\t"
    "ds_read_b128 a[144:147], v240 offset:20480\n\t"
    "ds_read_b128 a[208:211], v240 offset:28672\n\t"
    "ds_read_b128 a[212:215], v240 offset:29696\n\t"
    "ds_read_b128 a[216:219], v240 offset:30720\n\t"
    "ds_read_b128 a[220:223], v240 offset:31744\n\t"
    "v_exp_f32 v160, v160\n\t"
    "v_exp_f32 v161, v161\n\t"
    "v_exp_f32 v162, v162\n\t"
    "v_exp_f32 v163, v163\n\t"
    "v_exp_f32 v168, v168\n\t"
    "v_exp_f32 v169, v169\n\t"
    "v_exp_f32 v170, v170\n\t"
    "v_exp_f32 v171, v171\n\t"
    "v_exp_f32 v176, v176\n\t"
    "v_exp_f32 v177, v177\n\t"
    "v_exp_f32 v178, v178\n\t"
    "v_exp_f32 v179, v179\n\t"
    "v_exp_f32 v184, v184\n\t"
    "v_exp_f32 v185, v185\n\t"
    "v_exp_f32 v186, v186\n\t"
    "v_exp_f32 v187, v187\n\t"
    "v_exp_f32 v164, v164\n\t"
    "v_exp_f32 v165, v165\n\t"
    "v_exp_f32 v166, v166\n\t"
    "v_exp_f32 v167, v167\n\t"
    "v_exp_f32 v172, v172\n\t"
    "v_exp_f32 v173, v173\n\t"
    "v_exp_f32 v174, v174\n\t"
    "v_exp_f32 v175, v175\n\t"
    "v_exp_f32 v180, v180\n\t"
    "v_exp_f32 v181, v181\n\t"
    "v_exp_f32 v182, v182\n\t"
    "v_exp_f32 v183, v183\n\t"
    "v_exp_f32 v188, v188\n\t"
    "v_exp_f32 v189, v189\n\t"
    "v_exp_f32 v190, v190\n\t"
    "v_exp_f32 v191, v191\n\t"
    "v_mov_b32 v246, v136\n\t"
    "v_mov_b32 v247, v137\n\t"
    "v_add_f32 v246, v246, v138\n\t"
    "v_add_f32 v247, v247, v139\n\t"
    "v_mov_b32 v248, v144\n\t"
    "v_mov_b32 v249, v145\n\t"
    "v_add_f32 v248, v248, v146\n\t"
    "v_add_f32 v249, v249, v147\n\t"
    "v_mov_b32 v250, v152\n\t"
    "v_mov_b32 v251, v153\n\t"
    "v_add_f32 v250, v250, v154\n\t"
    "v_add_f32 v251, v251, v155\n\t"
    "v_add_f32 v246, v246, v140\n\t"
    "v_add_f32 v247, v247, v141\n\t"
    "v_add_f32 v246, v246, v142\n\t"
    "v_add_f32 v247, v247, v143\n\t"
    "v_add_f32 v248, v248, v148\n\t"
    "v_add_f32 v249, v249, v149\n\t"
    "v_add_f32 v250, v250, v156\n\t"
    "v_add_f32 v251, v251, v157\n\t"
    "v_cvt_pk_bf16_f32 v192, v128, v129\n\t"
    "v_cvt_pk_bf16_f32 v193, v130, v131\n\t"
    "s_waitcnt lgkmcnt(0)\n\t"
    "s_nop 1\n\t"
    "s_branch L_slot1_%=\n\t"
    "L_bump_%=:\n\t"
    "s_add_u32 s60, s52, 1\n\t"
    "v_mov_b32 v252, s60\n\t"
    "v_mov_b32 v253, s53\n\t"
    "v_mov_b32 v254, 6144\n\t"
    "s_mov_b64 exec, 1\n\t"
    "global_store_dwordx2 v254, v[252:253], s[26:27] sc1\n\t"
    "s_mov_b64 exec, -1\n\t"
    "s_nop 3\n\t"
    "s_branch L_bumpret_%=\n\t"
    ".p2align 6\n\t"
    "L_top_%=:\n\t"
    "v_mfma_f32_16x16x32_bf16 v[128:131], a[128:131], a[0:3], v[224:227]\n\t"
    "v_cvt_pk_bf16_f32 v196, v136, v137\n\t"
    "v_add_f32 v250, v250, v158\n\t"
    "v_mfma_f32_16x16x32_bf16 v[128:131], a[132:135], a[4:7], v[128:131]\n\t"
    "v_cvt_pk_bf16_f32 v197, v138, v139\n\t"
    "v_add_f32 v251, v251, v159\n\t"
    "v_mfma_f32_16x16x32_bf16 v[128:131], a[136:139], a[8:11], v[128:131]\n\t"
    "v_cvt_pk_bf16_f32 v200, v144, v145\n\t"
    "v_add_f32 v246, v246, v168\n\t"
    "v_mfma_f32_16x16x32_bf16 v[128:131], a[140:143], a[12:15], v[128:131]\n\t"
    "v_cvt_pk_bf16_f32 v201, v146, v147\n\t"
    "v_add_f32 v247, v247, v169\n\t"
    "v_mfma_f32_16x16x32_bf16 v[136:139], a[140:143], a[28:31], v[228:231]\n\t"
    "v_cvt_pk_bf16_f32 v204, v152, v153\n\t"
    "v_add_f32 v246, v246, v170\n\t"
    "v_mfma_f32_16x16x32_bf16 v[136:139], a[136:139], a[24:27], v[136:139]\n\t"
    "v_cvt_pk_bf16_f32 v205, v154, v155\n\t"
    "v_add_f32 v247, v247, v171\n\t"
    "v_mfma_f32_16x16x32_bf16 v[136:139], a[132:135], a[20:23], v[136:139]\n\t"
    "v_cvt_pk_bf16_f32 v194, v132, v133\n\t"
    "v_add_f32 v248, v248, v176\n\t"
    "v_mfma_f32_16x16x32_bf16 v[136:139], a[128:131], a[16:19], v[136:139]\n\t"
    "v_cvt_pk_bf16_f32 v195, v134, v135\n\t"
    "v_add_f32 v249, v249, v177\n\t"
    "v_mfma_f32_16x16x32_bf16 v[144:147], a[128:131], a[32:35], v[232:235]\n\t"
    "v_cvt_pk_bf16_f32 v198, v140, v141\n\t"
    "v_add_f32 v248, v248, v178\n\t"
    "v_mfma_f32_16x16x32_bf16 v[144:147], a[132:135], a[36:39], v[144:147]\n\t"
    "v_xor_b32 v240, 0x10000, v240\n\t"
    "v_add_f32 v249, v249, v179\n\t"
    "v_mfma_f32_16x16x32_bf16 v[144:147], a[136:139], a[40:43], v[144:147]\n\t"
    "s_waitcnt vmcnt(0) lgkmcnt(0)\n\t"
    "v_cvt_pk_bf16_f32 v199, v142, v143\n\t"
    "v_add_f32 v248, v248, v150\n\t"
    "v_mfma_f32_16x16x32_bf16 v[144:147], a[140:143], a[44:47], v[144:147]\n\t"
    "s_barrier\n\t"
    "v_cvt_pk_bf16_f32 v202, v148, v149\n\t"
    "v_add_f32 v249, v249, v151\n\t"
    "v_mfma_f32_16x16x32_bf16 v[152:155], a[140:143], a[60:63], v[236:239]\n\t"
    "v_cvt_pk_bf16_f32 v203, v150, v151\n\t"
    "v_add_f32 v250, v250, v184\n\t"
    "v_mfma_f32_16x16x32_bf16 v[152:155], a[136:139], a[56:59], v[152:155]\n\t"
    "ds_read_b128 a[140:143], v240 offset:3072\n\t"
    "v_cvt_pk_bf16_f32 v206, v156, v157\n\t"
    "v_mfma_f32_16x16x32_bf16 v[152:155], a[132:135], a[52:55], v[152:155]\n\t"
    "v_cvt_pk_bf16_f32 v207, v158, v159\n\t"
    "v_add_f32 v251, v251, v185\n\t"
    "v_mfma_f32_16x16x32_bf16 v[152:155], a[128:131], a[48:51], v[152:155]\n\t"
    "v_cvt_pk_bf16_f32 v208, v160, v161\n\t"
    "v_add_f32 v250, v250, v186\n\t"
    "v_mfma_f32_16x16x32_bf16 v[132:135], a[144:147], a[0:3], v[224:227]\n\t"
    "ds_read_b128 a[128:131], v240 offset:0\n\t"
    "v_cvt_pk_bf16_f32 v209, v162, v163\n\t"
    "v_mfma_f32_16x16x32_bf16 v[132:135], a[148:151], a[4:7], v[132:135]\n\t"
    "v_cvt_pk_bf16_f32 v212, v168, v169\n\t"
    "v_add_f32 v251, v251, v187\n\t"
    "v_mfma_f32_16x16x32_bf16 v[132:135], a[152:155], a[8:11], v[132:135]\n\t"
    "v_cvt_pk_bf16_f32 v213, v170, v171\n\t"
    "v_add_f32 v246, v246, v172\n\t"
    "v_mfma_f32_16x16x32_bf16 v[132:135], a[156:159], a[12:15], v[132:135]\n\t"
    "ds_read_b128 a[132:135], v240 offset:1024\n\t"
    "v_cvt_pk_bf16_f32 v216, v176, v177\n\t"
    "v_mfma_f32_16x16x32_bf16 v[140:143], a[156:159], a[28:31], v[228:231]\n\t"
    "v_cvt_pk_bf16_f32 v217, v178, v179\n\t"
    "v_add_f32 v247, v247, v173\n\t"
    "v_mfma_f32_16x16x32_bf16 v[140:143], a[152:155], a[24:27], v[140:143]\n\t"
    "v_cvt_pk_bf16_f32 v220, v184, v185\n\t"
    "v_add_f32 v246, v246, v174\n\t"
    "v_mfma_f32_16x16x32_bf16 v[140:143], a[148:151], a[20:23], v[140:143]\n\t"
    "ds_read_b128 a[136:139], v240 offset:2048\n\t"
    "v_cvt_pk_bf16_f32 v221, v186, v187\n\t"
    "v_mfma_f32_16x16x32_bf16 v[140:143], a[144:147], a[16:19], v[140:143]\n\t"
    "s_add_u32 s65, s56, 0\n\t"
    "v_add_f32 v247, v247, v175\n\t"
    "v_add_f32 v248, v248, v180\n\t"
    "v_mfma_f32_16x16x32_bf16 v[148:151], a[144:147], a[32:35], v[232:235]\n\t"
    "s_add_u32 m0, s57, 0\n\t"
    "v_add_f32 v249, v249, v181\n\t"
    "v_add_f32 v248, v248, v182\n\t"
    "v_mfma_f32_16x16x32_bf16 v[148:151], a[148:151], a[36:39], v[148:151]\n\t"
    "buffer_load_dwordx4 v241, s[40:43], s65 offen offset:0 sc1 lds\n\t"
    "v_mfma_f32_16x16x32_bf16 v[148:151], a[152:155], a[40:43], v[148:151]\n\t"
    "v_add_f32 v249, v249, v183\n\t"
    "v_add_f32 v250, v250, v188\n\t"
    "v_mfma_f32_16x16x32_bf16 v[148:151], a[156:159], a[44:47], v[148:151]\n\t"
    "buffer_load_dwordx4 v241, s[40:43], s65 offen offset:1024 sc1 lds\n\t"
    "v_mfma_f32_16x16x32_bf16 v[156:159], a[156:159], a[60:63], v[236:239]\n\t"
    "v_add_f32 v251, v251, v189\n\t"
    "v_add_f32 v250, v250, v190\n\t"
    "v_mfma_f32_16x16x32_bf16 v[156:159], a[152:155], a[56:59], v[156:159]\n\t"
    "ds_read_b128 a[156:159], v240 offset:7168\n\t"
    "v_cvt_pk_bf16_f32 v210, v164, v165\n\t"
    "v_mfma_f32_16x16x32_bf16 v[156:159], a[148:151], a[52:55], v[156:159]\n\t"
    "v_add_f32 v251, v251, v191\n\t"
    "v_mfma_f32_16x16x32_bf16 v[156:159], a[144:147], a[48:51], v[156:159]\n\t"
    "s_waitcnt lgkmcnt(2)\n\t"
    "buffer_load_dwordx4 v241, s[40:43], s65 offen offset:2048 sc1 lds\n\t"
    "v_mfma_f32_16x16x32_bf16 v[160:163], a[128:131], a[0:3], v[224:227]\n\t"
    "ds_read_b128 a[144:147], v240 offset:4096\n\t"
    "v_cvt_pk_bf16_f32 v211, v166, v167\n\t"
    "v_mfma_f32_16x16x32_bf16 v[160:163], a[132:135], a[4:7], v[160:163]\n\t"
    "s_waitcnt lgkmcnt(2)\n\t"
    "v_exp_f32 v128, v128\n\t"
    "v_mfma_f32_16x16x32_bf16 v[160:163], a[136:139], a[8:11], v[160:163]\n\t"
    "buffer_load_dwordx4 v241, s[40:43], s65 offen offset:3072 sc1 lds\n\t"
    "v_mfma_f32_16x16x32_bf16 v[160:163], a[140:143], a[12:15], v[160:163]\n\t"
    "ds_read_b128 a[148:151], v240 offset:5120\n\t"
    "v_cvt_pk_bf16_f32 v214, v172, v173\n\t"
    "v_mfma_f32_16x16x32_bf16 v[168:171], a[140:143], a[28:31], v[228:231]\n\t"
    "v_exp_f32 v129, v129\n\t"
    "v_mfma_f32_16x16x32_bf16 v[168:171], a[136:139], a[24:27], v[168:171]\n\t"
    "v_exp_f32 v130, v130\n\t"
    "v_mfma_f32_16x16x32_bf16 v[168:171], a[132:135], a[20:23], v[168:171]\n\t"
    "ds_read_b128 a[152:155], v240 offset:6144\n\t"
    "v_cvt_pk_bf16_f32 v215, v174, v175\n\t"
    "v_mfma_f32_16x16x32_bf16 v[168:171], a[128:131], a[16:19], v[168:171]\n\t"
    "s_add_u32 s65, s56, 4096\n\t"
    "v_exp_f32 v131, v131\n\t"
    "v_mfma_f32_16x16x32_bf16 v[176:179], a[128:131], a[32:35], v[232:235]\n\t"
    "s_add_u32 m0, s57, 4096\n\t"
    "v_exp_f32 v136, v136\n\t"
    "v_mfma_f32_16x16x32_bf16 v[176:179], a[132:135], a[36:39], v[176:179]\n\t"
    "buffer_load_dwordx4 v241, s[40:43], s65 offen offset:0 sc1 lds\n\t"
    "v_mfma_f32_16x16x32_bf16 v[176:179], a[136:139], a[40:43], v[176:179]\n\t"
    "v_exp_f32 v137, v137\n\t"
    "v_mfma_f32_16x16x32_bf16 v[176:179], a[140:143], a[44:47], v[176:179]\n\t"
    "buffer_load_dwordx4 v241, s[40:43], s65 offen offset:1024 sc1 lds\n\t"
    "v_mfma_f32_16x16x32_bf16 v[184:187], a[140:143], a[60:63], v[236:239]\n\t"
    "v_exp_f32 v138, v138\n\t"
    "v_mfma_f32_16x16x32_bf16 v[184:187], a[136:139], a[56:59], v[184:187]\n\t"
    "ds_read_b128 a[140:143], v240 offset:19456\n\t"
    "v_cvt_pk_bf16_f32 v218, v180, v181\n\t"
    "v_mfma_f32_16x16x32_bf16 v[184:187], a[132:135], a[52:55], v[184:187]\n\t"
    "v_exp_f32 v139, v139\n\t"
    "v_mfma_f32_16x16x32_bf16 v[184:187], a[128:131], a[48:51], v[184:187]\n\t"
    "s_waitcnt lgkmcnt(2)\n\t"
    "buffer_load_dwordx4 v241, s[40:43], s65 offen offset:2048 sc1 lds\n\t"
    "v_mfma_f32_16x16x32_bf16 v[164:167], a[144:147], a[0:3], v[224:227]\n\t"
    "v_exp_f32 v144, v144\n\t"
    "v_mfma_f32_16x16x32_bf16 v[164:167], a[148:151], a[4:7], v[164:167]\n\t"
    "s_waitcnt lgkmcnt(1)\n\t"
    "ds_read_b128 a[128:131], v240 offset:16384\n\t"
    "v_cvt_pk_bf16_f32 v219, v182, v183\n\t"
    "v_mfma_f32_16x16x32_bf16 v[164:167], a[152:155], a[8:11], v[164:167]\n\t"
    "v_exp_f32 v145, v145\n\t"
    "v_mfma_f32_16x16x32_bf16 v[164:167], a[156:159], a[12:15], v[164:167]\n\t"
    "buffer_load_dwordx4 v241, s[40:43], s65 offen offset:3072 sc1 lds\n\t"
    "v_mfma_f32_16x16x32_bf16 v[172:175], a[156:159], a[28:31], v[228:231]\n\t"
    "v_exp_f32 v146, v146\n\t"
    "v_mfma_f32_16x16x32_bf16 v[172:175], a[152:155], a[24:27], v[172:175]\n\t"
    "ds_read_b128 a[132:135], v240 offset:17408\n\t"
    "v_cvt_pk_bf16_f32 v222, v188, v189\n\t"
    "v_mfma_f32_16x16x32_bf16 v[172:175], a[148:151], a[20:23], v[172:175]\n\t"
    "v_exp_f32 v147, v147\n\t"
    "v_mfma_f32_16x16x32_bf16 v[172:175], a[144:147], a[16:19], v[172:175]\n\t"
    "v_exp_f32 v152, v152\n\t"
    "v_mfma_f32_16x16x32_bf16 v[180:183], a[144:147], a[32:35], v[232:235]\n\t"
    "v_exp_f32 v153, v153\n\t"
    "v_mfma_f32_16x16x32_bf16 v[180:183], a[148:151], a[36:39], v[180:183]\n\t"
    "ds_read_b128 a[136:139], v240 offset:18432\n\t"
    "v_cvt_pk_bf16_f32 v223, v190, v191\n\t"
    "v_mfma_f32_16x16x32_bf16 v[180:183], a[152:155], a[40:43], v[180:183]\n\t"
    "v_exp_f32 v154, v154\n\t"
    "v_mfma_f32_16x16x32_bf16 v[180:183], a[156:159], a[44:47], v[180:183]\n\t"
    "v_exp_f32 v155, v155\n\t"
    "v_mfma_f32_16x16x32_bf16 v[188:191], a[156:159], a[60:63], v[236:239]\n\t"
    "v_mfma_f32_16x16x32_bf16 v[188:191], a[152:155], a[56:59], v[188:191]\n\t"
    "ds_read_b128 a[156:159], v240 offset:23552\n\t"
    "v_add_f32 v246, v246, v136\n\t"
    "v_mfma_f32_16x16x32_bf16 v[188:191], a[148:151], a[52:55], v[188:191]\n\t"
    "v_exp_f32 v132, v132\n\t"
    "v_mfma_f32_16x16x32_bf16 v[188:191], a[144:147], a[48:51], v[188:191]\n\t"
    "v_exp_f32 v133, v133\n\t"
    "v_mfma_f32_16x16x32_bf16 a[228:231], v[192:195], a[224:227], a[228:231]\n\t"
    "v_exp_f32 v134, v134\n\t"
    "v_mfma_f32_16x16x32_bf16 a[228:231], v[208:211], a[224:227], a[228:231]\n\t"
    "ds_read_b128 a[144:147], v240 offset:20480\n\t"
    "v_add_f32 v247, v247, v137\n\t"
    "v_mfma_f32_16x16x32_bf16 v[0:3], v[208:211], a[192:195], v[0:3]\n\t"
    "v_exp_f32 v135, v135\n\t"
    "v_mfma_f32_16x16x32_bf16 v[0:3], v[192:195], a[160:163], v[0:3]\n\t"
    "v_exp_f32 v140, v140\n\t"
    "v_mfma_f32_16x16x32_bf16 v[16:19], v[192:195], a[164:167], v[16:19]\n\t"
    "v_exp_f32 v141, v141\n\t"
    "v_mfma_f32_16x16x32_bf16 v[16:19], v[208:211], a[196:199], v[16:19]\n\t"
    "ds_read_b128 a[148:151], v240 offset:21504\n\t"
    "v_add_f32 v246, v246, v138\n\t"
    "v_mfma_f32_16x16x32_bf16 v[32:35], v[208:211], a[200:203], v[32:35]\n\t"
    "v_exp_f32 v142, v142\n\t"
    "v_mfma_f32_16x16x32_bf16 v[32:35], v[192:195], a[168:171], v[32:35]\n\t"
    "v_exp_f32 v143, v143\n\t"
    "v_mfma_f32_16x16x32_bf16 v[48:51], v[192:195], a[172:175], v[48:51]\n\t"
    "v_exp_f32 v148, v148\n\t"
    "v_mfma_f32_16x16x32_bf16 v[48:51], v[208:211], a[204:207], v[48:51]\n\t"
    "ds_read_b128 a[152:155], v240 offset:22528\n\t"
    "v_add_f32 v247, v247, v139\n\t"
    "v_mfma_f32_16x16x32_bf16 v[64:67], v[208:211], a[208:211], v[64:67]\n\t"
    "v_exp_f32 v149, v149\n\t"
    "v_mfma_f32_16x16x32_bf16 v[64:67], v[192:195], a[176:179], v[64:67]\n\t"
    "v_exp_f32 v150, v150\n\t"
    "v_mfma_f32_16x16x32_bf16 v[80:83], v[192:195], a[180:183], v[80:83]\n\t"
    "v_exp_f32 v151, v151\n\t"
    "v_mfma_f32_16x16x32_bf16 v[80:83], v[208:211], a[212:215], v[80:83]\n\t"
    "v_exp_f32 v156, v156\n\t"
    "v_mfma_f32_16x16x32_bf16 v[96:99], v[208:211], a[216:219], v[96:99]\n\t"
    "v_exp_f32 v157, v157\n\t"
    "v_mfma_f32_16x16x32_bf16 v[96:99], v[192:195], a[184:187], v[96:99]\n\t"
    "v_exp_f32 v158, v158\n\t"
    "v_mfma_f32_16x16x32_bf16 v[112:115], v[192:195], a[188:191], v[112:115]\n\t"
    "v_exp_f32 v159, v159\n\t"
    "v_mfma_f32_16x16x32_bf16 v[112:115], v[208:211], a[220:223], v[112:115]\n\t"
    "v_exp_f32 v160, v160\n\t"
    "v_mfma_f32_16x16x32_bf16 v[4:7], v[196:199], a[160:163], v[4:7]\n\t"
    "v_exp_f32 v161, v161\n\t"
    "v_mfma_f32_16x16x32_bf16 v[4:7], v[212:215], a[192:195], v[4:7]\n\t"
    "v_exp_f32 v162, v162\n\t"
    "v_mfma_f32_16x16x32_bf16 v[20:23], v[212:215], a[196:199], v[20:23]\n\t"
    "v_exp_f32 v163, v163\n\t"
    "v_mfma_f32_16x16x32_bf16 v[20:23], v[196:199], a[164:167], v[20:23]\n\t"
    "v_exp_f32 v168, v168\n\t"
    "v_mfma_f32_16x16x32_bf16 v[36:39], v[196:199], a[168:171], v[36:39]\n\t"
    "v_exp_f32 v169, v169\n\t"
    "v_mfma_f32_16x16x32_bf16 v[36:39], v[212:215], a[200:203], v[36:39]\n\t"
    "v_exp_f32 v170, v170\n\t"
    "v_mfma_f32_16x16x32_bf16 v[52:55], v[212:215], a[204:207], v[52:55]\n\t"
    "v_exp_f32 v171, v171\n\t"
    "v_mfma_f32_16x16x32_bf16 v[52:55], v[196:199], a[172:175], v[52:55]\n\t"
    "v_exp_f32 v176, v176\n\t"
    "v_mfma_f32_16x16x32_bf16 v[68:71], v[196:199], a[176:179], v[68:71]\n\t"
    "v_exp_f32 v177, v177\n\t"
    "v_mfma_f32_16x16x32_bf16 v[68:71], v[212:215], a[208:211], v[68:71]\n\t"
    "v_exp_f32 v178, v178\n\t"
    "v_mfma_f32_16x16x32_bf16 v[84:87], v[212:215], a[212:215], v[84:87]\n\t"
    "v_exp_f32 v179, v179\n\t"
    "v_mfma_f32_16x16x32_bf16 v[84:87], v[196:199], a[180:183], v[84:87]\n\t"
    "v_exp_f32 v184, v184\n\t"
    "v_mfma_f32_16x16x32_bf16 v[100:103], v[196:199], a[184:187], v[100:103]\n\t"
    "v_exp_f32 v185, v185\n\t"
    "v_mfma_f32_16x16x32_bf16 v[100:103], v[212:215], a[216:219], v[100:103]\n\t"
    "v_exp_f32 v186, v186\n\t"
    "v_mfma_f32_16x16x32_bf16 v[116:119], v[212:215], a[220:223], v[116:119]\n\t"
    "v_exp_f32 v187, v187\n\t"
    "v_mfma_f32_16x16x32_bf16 v[116:119], v[196:199], a[188:191], v[116:119]\n\t"
    "v_add_f32 v250, v250, v156\n\t"
    "v_add_f32 v251, v251, v157\n\t"
    "v_mfma_f32_16x16x32_bf16 v[8:11], v[200:203], a[160:163], v[8:11]\n\t"
    "v_exp_f32 v164, v164\n\t"
    "v_mfma_f32_16x16x32_bf16 v[8:11], v[216:219], a[192:195], v[8:11]\n\t"
    "v_exp_f32 v165, v165\n\t"
    "v_mfma_f32_16x16x32_bf16 v[24:27], v[216:219], a[196:199], v[24:27]\n\t"
    "v_exp_f32 v166, v166\n\t"
    "v_mfma_f32_16x16x32_bf16 v[24:27], v[200:203], a[164:167], v[24:27]\n\t"
    "v_exp_f32 v167, v167\n\t"
    "v_mfma_f32_16x16x32_bf16 v[40:43], v[200:203], a[168:171], v[40:43]\n\t"
    "v_exp_f32 v172, v172\n\t"
    "v_mfma_f32_16x16x32_bf16 v[40:43], v[216:219], a[200:203], v[40:43]\n\t"
    "v_exp_f32 v173, v173\n\t"
    "v_mfma_f32_16x16x32_bf16 v[56:59], v[216:219], a[204:207], v[56:59]\n\t"
    "v_exp_f32 v174, v174\n\t"
    "v_mfma_f32_16x16x32_bf16 v[56:59], v[200:203], a[172:175], v[56:59]\n\t"
    "v_exp_f32 v175, v175\n\t"
    "v_mfma_f32_16x16x32_bf16 v[72:75], v[200:203], a[176:179], v[72:75]\n\t"
    "v_exp_f32 v180, v180\n\t"
    "v_mfma_f32_16x16x32_bf16 v[72:75], v[216:219], a[208:211], v[72:75]\n\t"
    "v_exp_f32 v181, v181\n\t"
    "v_mfma_f32_16x16x32_bf16 v[88:91], v[216:219], a[212:215], v[88:91]\n\t"
    "v_exp_f32 v182, v182\n\t"
    "v_mfma_f32_16x16x32_bf16 v[88:91], v[200:203], a[180:183], v[88:91]\n\t"
    "v_exp_f32 v183, v183\n\t"
    "v_mfma_f32_16x16x32_bf16 v[104:107], v[200:203], a[184:187], v[104:107]\n\t"
    "v_exp_f32 v188, v188\n\t"
    "v_mfma_f32_16x16x32_bf16 v[104:107], v[216:219], a[216:219], v[104:107]\n\t"
    "v_exp_f32 v189, v189\n\t"
    "v_mfma_f32_16x16x32_bf16 v[120:123], v[216:219], a[220:223], v[120:123]\n\t"
    "v_exp_f32 v190, v190\n\t"
    "v_mfma_f32_16x16x32_bf16 v[120:123], v[200:203], a[188:191], v[120:123]\n\t"
    "v_exp_f32 v191, v191\n\t"
    "v_mfma_f32_16x16x32_bf16 v[12:15], v[204:207], a[160:163], v[12:15]\n\t"
    "ds_read_b128 a[160:163], v240 offset:8192\n\t"
    "v_add_f32 v248, v248, v144\n\t"
    "v_mfma_f32_16x16x32_bf16 v[12:15], v[220:223], a[192:195], v[12:15]\n\t"
    "ds_read_b128 a[192:195], v240 offset:24576\n\t"
    "v_add_f32 v249, v249, v145\n\t"
    "v_mfma_f32_16x16x32_bf16 v[28:31], v[220:223], a[196:199], v[28:31]\n\t"
    "ds_read_b128 a[196:199], v240 offset:25600\n\t"
    "v_add_f32 v248, v248, v146\n\t"
    "v_mfma_f32_16x16x32_bf16 v[28:31], v[204:207], a[164:167], v[28:31]\n\t"
    "ds_read_b128 a[164:167], v240 offset:9216\n\t"
    "v_add_f32 v249, v249, v147\n\t"
    "v_mfma_f32_16x16x32_bf16 v[44:47], v[204:207], a[168:171], v[44:47]\n\t"
    "ds_read_b128 a[168:171], v240 offset:10240\n\t"
    "v_add_f32 v250, v250, v152\n\t"
    "v_mfma_f32_16x16x32_bf16 v[44:47], v[220:223], a[200:203], v[44:47]\n\t"
    "ds_read_b128 a[200:203], v240 offset:26624\n\t"
    "v_add_f32 v251, v251, v153\n\t"
    "v_mfma_f32_16x16x32_bf16 v[60:63], v[220:223], a[204:207], v[60:63]\n\t"
    "ds_read_b128 a[204:207], v240 offset:27648\n\t"
    "v_add_f32 v250, v250, v154\n\t"
    "v_mfma_f32_16x16x32_bf16 v[60:63], v[204:207], a[172:175], v[60:63]\n\t"
    "ds_read_b128 a[172:175], v240 offset:11264\n\t"
    "v_add_f32 v251, v251, v155\n\t"
    "v_mfma_f32_16x16x32_bf16 v[76:79], v[204:207], a[176:179], v[76:79]\n\t"
    "ds_read_b128 a[176:179], v240 offset:12288\n\t"
    "v_add_f32 v246, v246, v140\n\t"
    "v_mfma_f32_16x16x32_bf16 v[76:79], v[220:223], a[208:211], v[76:79]\n\t"
    "ds_read_b128 a[208:211], v240 offset:28672\n\t"
    "v_add_f32 v247, v247, v141\n\t"
    "v_mfma_f32_16x16x32_bf16 v[92:95], v[220:223], a[212:215], v[92:95]\n\t"
    "ds_read_b128 a[212:215], v240 offset:29696\n\t"
    "v_add_f32 v246, v246, v142\n\t"
    "v_mfma_f32_16x16x32_bf16 v[92:95], v[204:207], a[180:183], v[92:95]\n\t"
    "ds_read_b128 a[180:183], v240 offset:13312\n\t"
    "v_add_f32 v247, v247, v143\n\t"
    "v_mfma_f32_16x16x32_bf16 v[108:111], v[204:207], a[184:187], v[108:111]\n\t"
    "ds_read_b128 a[184:187], v240 offset:14336\n\t"
    "v_add_f32 v248, v248, v148\n\t"
    "v_mfma_f32_16x16x32_bf16 v[108:111], v[220:223], a[216:219], v[108:111]\n\t"
    "ds_read_b128 a[216:219], v240 offset:30720\n\t"
    "v_add_f32 v249, v249, v149\n\t"
    "v_mfma_f32_16x16x32_bf16 v[124:127], v[220:223], a[220:223], v[124:127]\n\t"
    "ds_read_b128 a[220:223], v240 offset:31744\n\t"
    "v_cvt_pk_bf16_f32 v192, v128, v129\n\t"
    "v_mfma_f32_16x16x32_bf16 v[124:127], v[204:207], a[188:191], v[124:127]\n\t"
    "s_waitcnt lgkmcnt(11)\n\t"
    "ds_read_b128 a[188:191], v240 offset:15360\n\t"
    "v_cvt_pk_bf16_f32 v193, v130, v131\n\t"
    "L_slot1_%=:\n\t"
    "v_mfma_f32_16x16x32_bf16 v[128:131], a[128:131], a[0:3], v[224:227]\n\t"
    "v_cvt_pk_bf16_f32 v196, v136, v137\n\t"
    "v_add_f32 v250, v250, v158\n\t"
    "v_mfma_f32_16x16x32_bf16 v[128:131], a[132:135], a[4:7], v[128:131]\n\t"
    "s_cmp_eq_u32 s59, s64\n\t"
    "v_cvt_pk_bf16_f32 v197, v138, v139\n\t"
    "v_add_f32 v248, v248, v150\n\t"
    "v_mfma_f32_16x16x32_bf16 v[128:131], a[136:139], a[8:11], v[128:131]\n\t"
    "s_cbranch_scc1 L_bump_%=\n\t"
    "L_bumpret_%=:\n\t"
    "v_cvt_pk_bf16_f32 v200, v144, v145\n\t"
    "v_add_f32 v249, v249, v151\n\t"
    "v_mfma_f32_16x16x32_bf16 v[128:131], a[140:143], a[12:15], v[128:131]\n\t"
    "v_cvt_pk_bf16_f32 v201, v146, v147\n\t"
    "v_add_f32 v251, v251, v159\n\t"
    "v_mfma_f32_16x16x32_bf16 v[136:139], a[140:143], a[28:31], v[228:231]\n\t"
    "v_cvt_pk_bf16_f32 v204, v152, v153\n\t"
    "v_add_f32 v246, v246, v168\n\t"
    "v_mfma_f32_16x16x32_bf16 v[136:139], a[136:139], a[24:27], v[136:139]\n\t"
    "v_cvt_pk_bf16_f32 v205, v154, v155\n\t"
    "v_add_f32 v247, v247, v169\n\t"
    "v_mfma_f32_16x16x32_bf16 v[136:139], a[132:135], a[20:23], v[136:139]\n\t"
    "v_cvt_pk_bf16_f32 v194, v132, v133\n\t"
    "v_add_f32 v246, v246, v170\n\t"
    "v_mfma_f32_16x16x32_bf16 v[136:139], a[128:131], a[16:19], v[136:139]\n\t"
    "v_cvt_pk_bf16_f32 v195, v134, v135\n\t"
    "v_add_f32 v247, v247, v171\n\t"
    "v_mfma_f32_16x16x32_bf16 v[144:147], a[128:131], a[32:35], v[232:235]\n\t"
    "v_cvt_pk_bf16_f32 v198, v140, v141\n\t"
    "v_add_f32 v248, v248, v176\n\t"
    "v_mfma_f32_16x16x32_bf16 v[144:147], a[132:135], a[36:39], v[144:147]\n\t"
    "v_cvt_pk_bf16_f32 v199, v142, v143\n\t"
    "v_add_f32 v249, v249, v177\n\t"
    "v_mfma_f32_16x16x32_bf16 v[144:147], a[136:139], a[40:43], v[144:147]\n\t"
    "v_cvt_pk_bf16_f32 v202, v148, v149\n\t"
    "v_add_f32 v248, v248, v178\n\t"
    "v_mfma_f32_16x16x32_bf16 v[144:147], a[140:143], a[44:47], v[144:147]\n\t"
    "v_cvt_pk_bf16_f32 v203, v150, v151\n\t"
    "v_add_f32 v249, v249, v179\n\t"
    "v_mfma_f32_16x16x32_bf16 v[152:155], a[140:143], a[60:63], v[236:239]\n\t"
    "v_cvt_pk_bf16_f32 v206, v156, v157\n\t"
    "v_add_f32 v250, v250, v184\n\t"
    "v_mfma_f32_16x16x32_bf16 v[152:155], a[136:139], a[56:59], v[152:155]\n\t"
    "ds_read_b128 a[140:143], v240 offset:35840\n\t"
    "v_cvt_pk_bf16_f32 v207, v158, v159\n\t"
    "v_mfma_f32_16x16x32_bf16 v[152:155], a[132:135], a[52:55], v[152:155]\n\t"
    "v_cvt_pk_bf16_f32 v208, v160, v161\n\t"
    "v_add_f32 v251, v251, v185\n\t"
    "v_mfma_f32_16x16x32_bf16 v[152:155], a[128:131], a[48:51], v[152:155]\n\t"
    "v_cvt_pk_bf16_f32 v209, v162, v163\n\t"
    "v_add_f32 v250, v250, v186\n\t"
    "v_mfma_f32_16x16x32_bf16 v[132:135], a[144:147], a[0:3], v[224:227]\n\t"
    "ds_read_b128 a[128:131], v240 offset:32768\n\t"
    "v_cvt_pk_bf16_f32 v212, v168, v169\n\t"
    "v_mfma_f32_16x16x32_bf16 v[132:135], a[148:151], a[4:7], v[132:135]\n\t"
    "v_cvt_pk_bf16_f32 v213, v170, v171\n\t"
    "v_add_f32 v251, v251, v187\n\t"
    "v_mfma_f32_16x16x32_bf16 v[132:135], a[152:155], a[8:11], v[132:135]\n\t"
    "v_cvt_pk_bf16_f32 v216, v176, v177\n\t"
    "v_add_f32 v246, v246, v172\n\t"
    "v_mfma_f32_16x16x32_bf16 v[132:135], a[156:159], a[12:15], v[132:135]\n\t"
    "ds_read_b128 a[132:135], v240 offset:33792\n\t"
    "v_cvt_pk_bf16_f32 v217, v178, v179\n\t"
    "v_mfma_f32_16x16x32_bf16 v[140:143], a[156:159], a[28:31], v[228:231]\n\t"
    "v_cvt_pk_bf16_f32 v220, v184, v185\n\t"
    "v_add_f32 v247, v247, v173\n\t"
    "v_mfma_f32_16x16x32_bf16 v[140:143], a[152:155], a[24:27], v[140:143]\n\t"
    "v_add_f32 v246, v246, v174\n\t"
    "v_add_f32 v247, v247, v175\n\t"
    "v_mfma_f32_16x16x32_bf16 v[140:143], a[148:151], a[20:23], v[140:143]\n\t"
    "ds_read_b128 a[136:139], v240 offset:34816\n\t"
    "v_cvt_pk_bf16_f32 v221, v186, v187\n\t"
    "v_mfma_f32_16x16x32_bf16 v[140:143], a[144:147], a[16:19], v[140:143]\n\t"
    "s_add_u32 s65, s56, 8192\n\t"
    "v_add_f32 v248, v248, v180\n\t"
    "v_add_f32 v249, v249, v181\n\t"
    "v_mfma_f32_16x16x32_bf16 v[148:151], a[144:147], a[32:35], v[232:235]\n\t"
    "s_add_u32 m0, s57, 8192\n\t"
    "v_add_f32 v248, v248, v182\n\t"
    "v_add_f32 v249, v249, v183\n\t"
    "v_mfma_f32_16x16x32_bf16 v[148:151], a[148:151], a[36:39], v[148:151]\n\t"
    "buffer_load_dwordx4 v241, s[40:43], s65 offen offset:0 sc1 lds\n\t"
    "v_mfma_f32_16x16x32_bf16 v[148:151], a[152:155], a[40:43], v[148:151]\n\t"
    "v_add_f32 v250, v250, v188\n\t"
    "v_add_f32 v251, v251, v189\n\t"
    "v_mfma_f32_16x16x32_bf16 v[148:151], a[156:159], a[44:47], v[148:151]\n\t"
    "buffer_load_dwordx4 v241, s[40:43], s65 offen offset:1024 sc1 lds\n\t"
    "v_mfma_f32_16x16x32_bf16 v[156:159], a[156:159], a[60:63], v[236:239]\n\t"
    "v_add_f32 v250, v250, v190\n\t"
    "v_add_f32 v251, v251, v191\n\t"
    "v_mfma_f32_16x16x32_bf16 v[156:159], a[152:155], a[56:59], v[156:159]\n\t"
    "ds_read_b128 a[156:159], v240 offset:39936\n\t"
    "v_cvt_pk_bf16_f32 v210, v164, v165\n\t"
    "v_mfma_f32_16x16x32_bf16 v[156:159], a[148:151], a[52:55], v[156:159]\n\t"
    "v_mfma_f32_16x16x32_bf16 v[156:159], a[144:147], a[48:51], v[156:159]\n\t"
    "s_waitcnt lgkmcnt(2)\n\t"
    "buffer_load_dwordx4 v241, s[40:43], s65 offen offset:2048 sc1 lds\n\t"
    "v_mfma_f32_16x16x32_bf16 v[160:163], a[128:131], a[0:3], v[224:227]\n\t"
    "ds_read_b128 a[144:147], v240 offset:36864\n\t"
    "v_cvt_pk_bf16_f32 v211, v166, v167\n\t"
    "v_mfma_f32_16x16x32_bf16 v[160:163], a[132:135], a[4:7], v[160:163]\n\t"
    "s_waitcnt lgkmcnt(2)\n\t"
    "v_exp_f32 v128, v128\n\t"
    "v_mfma_f32_16x16x32_bf16 v[160:163], a[136:139], a[8:11], v[160:163]\n\t"
    "buffer_load_dwordx4 v241, s[40:43], s65 offen offset:3072 sc1 lds\n\t"
    "v_mfma_f32_16x16x32_bf16 v[160:163], a[140:143], a[12:15], v[160:163]\n\t"
    "ds_read_b128 a[148:151], v240 offset:37888\n\t"
    "v_cvt_pk_bf16_f32 v214, v172, v173\n\t"
    "v_mfma_f32_16x16x32_bf16 v[168:171], a[140:143], a[28:31], v[228:231]\n\t"
    "v_exp_f32 v129, v129\n\t"
    "v_mfma_f32_16x16x32_bf16 v[168:171], a[136:139], a[24:27], v[168:171]\n\t"
    "v_exp_f32 v130, v130\n\t"
    "v_mfma_f32_16x16x32_bf16 v[168:171], a[132:135], a[20:23], v[168:171]\n\t"
    "ds_read_b128 a[152:155], v240 offset:38912\n\t"
    "v_cvt_pk_bf16_f32 v215, v174, v175\n\t"
    "v_mfma_f32_16x16x32_bf16 v[168:171], a[128:131], a[16:19], v[168:171]\n\t"
    "s_add_u32 s65, s56, 12288\n\t"
    "v_exp_f32 v131, v131\n\t"
    "v_mfma_f32_16x16x32_bf16 v[176:179], a[128:131], a[32:35], v[232:235]\n\t"
    "s_add_u32 m0, s57, 12288\n\t"
    "v_exp_f32 v136, v136\n\t"
    "v_mfma_f32_16x16x32_bf16 v[176:179], a[132:135], a[36:39], v[176:179]\n\t"
    "buffer_load_dwordx4 v241, s[40:43], s65 offen offset:0 sc1 lds\n\t"
    "v_mfma_f32_16x16x32_bf16 v[176:179], a[136:139], a[40:43], v[176:179]\n\t"
    "v_exp_f32 v137, v137\n\t"
    "v_mfma_f32_16x16x32_bf16 v[176:179], a[140:143], a[44:47], v[176:179]\n\t"
    "buffer_load_dwordx4 v241, s[40:43], s65 offen offset:1024 sc1 lds\n\t"
    "v_mfma_f32_16x16x32_bf16 v[184:187], a[140:143], a[60:63], v[236:239]\n\t"
    "v_exp_f32 v138, v138\n\t"
    "v_mfma_f32_16x16x32_bf16 v[184:187], a[136:139], a[56:59], v[184:187]\n\t"
    "ds_read_b128 a[140:143], v240 offset:52224\n\t"
    "v_cvt_pk_bf16_f32 v218, v180, v181\n\t"
    "v_mfma_f32_16x16x32_bf16 v[184:187], a[132:135], a[52:55], v[184:187]\n\t"
    "v_exp_f32 v139, v139\n\t"
    "v_mfma_f32_16x16x32_bf16 v[184:187], a[128:131], a[48:51], v[184:187]\n\t"
    "s_waitcnt lgkmcnt(2)\n\t"
    "buffer_load_dwordx4 v241, s[40:43], s65 offen offset:2048 sc1 lds\n\t"
    "v_mfma_f32_16x16x32_bf16 v[164:167], a[144:147], a[0:3], v[224:227]\n\t"
    "v_exp_f32 v144, v144\n\t"
    "v_mfma_f32_16x16x32_bf16 v[164:167], a[148:151], a[4:7], v[164:167]\n\t"
    "s_waitcnt lgkmcnt(1)\n\t"
    "ds_read_b128 a[128:131], v240 offset:49152\n\t"
    "v_cvt_pk_bf16_f32 v219, v182, v183\n\t"
    "v_mfma_f32_16x16x32_bf16 v[164:167], a[152:155], a[8:11], v[164:167]\n\t"
    "v_exp_f32 v145, v145\n\t"
    "v_mfma_f32_16x16x32_bf16 v[164:167], a[156:159], a[12:15], v[164:167]\n\t"
    "buffer_load_dwordx4 v241, s[40:43], s65 offen offset:3072 sc1 lds\n\t"
    "v_mfma_f32_16x16x32_bf16 v[172:175], a[156:159], a[28:31], v[228:231]\n\t"
    "v_exp_f32 v146, v146\n\t"
    "v_mfma_f32_16x16x32_bf16 v[172:175], a[152:155], a[24:27], v[172:175]\n\t"
    "ds_read_b128 a[132:135], v240 offset:50176\n\t"
    "v_cvt_pk_bf16_f32 v222, v188, v189\n\t"
    "v_mfma_f32_16x16x32_bf16 v[172:175], a[148:151], a[20:23], v[172:175]\n\t"
    "v_exp_f32 v147, v147\n\t"
    "v_mfma_f32_16x16x32_bf16 v[172:175], a[144:147], a[16:19], v[172:175]\n\t"
    "v_exp_f32 v152, v152\n\t"
    "v_mfma_f32_16x16x32_bf16 v[180:183], a[144:147], a[32:35], v[232:235]\n\t"
    "v_exp_f32 v153, v153\n\t"
    "v_mfma_f32_16x16x32_bf16 v[180:183], a[148:151], a[36:39], v[180:183]\n\t"
    "ds_read_b128 a[136:139], v240 offset:51200\n\t"
    "v_cvt_pk_bf16_f32 v223, v190, v191\n\t"
    "v_mfma_f32_16x16x32_bf16 v[180:183], a[152:155], a[40:43], v[180:183]\n\t"
    "v_exp_f32 v154, v154\n\t"
    "v_mfma_f32_16x16x32_bf16 v[180:183], a[156:159], a[44:47], v[180:183]\n\t"
    "v_exp_f32 v155, v155\n\t"
    "v_mfma_f32_16x16x32_bf16 v[188:191], a[156:159], a[60:63], v[236:239]\n\t"
    "v_mfma_f32_16x16x32_bf16 v[188:191], a[152:155], a[56:59], v[188:191]\n\t"
    "ds_read_b128 a[156:159], v240 offset:56320\n\t"
    "v_add_f32 v246, v246, v136\n\t"
    "v_mfma_f32_16x16x32_bf16 v[188:191], a[148:151], a[52:55], v[188:191]\n\t"
    "v_exp_f32 v132, v132\n\t"
    "v_mfma_f32_16x16x32_bf16 v[188:191], a[144:147], a[48:51], v[188:191]\n\t"
    "v_exp_f32 v133, v133\n\t"
    "v_mfma_f32_16x16x32_bf16 a[228:231], v[192:195], a[224:227], a[228:231]\n\t"
    "v_exp_f32 v134, v134\n\t"
    "v_mfma_f32_16x16x32_bf16 a[228:231], v[208:211], a[224:227], a[228:231]\n\t"
    "ds_read_b128 a[144:147], v240 offset:53248\n\t"
    "v_add_f32 v247, v247, v137\n\t"
    "v_mfma_f32_16x16x32_bf16 v[0:3], v[208:211], a[192:195], v[0:3]\n\t"
    "v_exp_f32 v135, v135\n\t"
    "v_mfma_f32_16x16x32_bf16 v[0:3], v[192:195], a[160:163], v[0:3]\n\t"
    "v_exp_f32 v140, v140\n\t"
    "v_mfma_f32_16x16x32_bf16 v[16:19], v[192:195], a[164:167], v[16:19]\n\t"
    "v_exp_f32 v141, v141\n\t"
    "v_mfma_f32_16x16x32_bf16 v[16:19], v[208:211], a[196:199], v[16:19]\n\t"
    "ds_read_b128 a[148:151], v240 offset:54272\n\t"
    "v_add_f32 v246, v246, v138\n\t"
    "v_mfma_f32_16x16x32_bf16 v[32:35], v[208:211], a[200:203], v[32:35]\n\t"
    "v_exp_f32 v142, v142\n\t"
    "v_mfma_f32_16x16x32_bf16 v[32:35], v[192:195], a[168:171], v[32:35]\n\t"
    "v_exp_f32 v143, v143\n\t"
    "v_mfma_f32_16x16x32_bf16 v[48:51], v[192:195], a[172:175], v[48:51]\n\t"
    "v_exp_f32 v148, v148\n\t"
    "v_mfma_f32_16x16x32_bf16 v[48:51], v[208:211], a[204:207], v[48:51]\n\t"
    "ds_read_b128 a[152:155], v240 offset:55296\n\t"
    "v_add_f32 v247, v247, v139\n\t"
    "v_mfma_f32_16x16x32_bf16 v[64:67], v[208:211], a[208:211], v[64:67]\n\t"
    "v_exp_f32 v149, v149\n\t"
    "v_mfma_f32_16x16x32_bf16 v[64:67], v[192:195], a[176:179], v[64:67]\n\t"
    "v_exp_f32 v150, v150\n\t"
    "v_mfma_f32_16x16x32_bf16 v[80:83], v[192:195], a[180:183], v[80:83]\n\t"
    "v_exp_f32 v151, v151\n\t"
    "v_mfma_f32_16x16x32_bf16 v[80:83], v[208:211], a[212:215], v[80:83]\n\t"
    "v_exp_f32 v156, v156\n\t"
    "v_mfma_f32_16x16x32_bf16 v[96:99], v[208:211], a[216:219], v[96:99]\n\t"
    "v_exp_f32 v157, v157\n\t"
    "v_mfma_f32_16x16x32_bf16 v[96:99], v[192:195], a[184:187], v[96:99]\n\t"
    "v_exp_f32 v158, v158\n\t"
    "v_mfma_f32_16x16x32_bf16 v[112:115], v[192:195], a[188:191], v[112:115]\n\t"
    "v_exp_f32 v159, v159\n\t"
    "v_mfma_f32_16x16x32_bf16 v[112:115], v[208:211], a[220:223], v[112:115]\n\t"
    "v_exp_f32 v160, v160\n\t"
    "v_mfma_f32_16x16x32_bf16 v[4:7], v[196:199], a[160:163], v[4:7]\n\t"
    "v_exp_f32 v161, v161\n\t"
    "v_mfma_f32_16x16x32_bf16 v[4:7], v[212:215], a[192:195], v[4:7]\n\t"
    "v_exp_f32 v162, v162\n\t"
    "v_mfma_f32_16x16x32_bf16 v[20:23], v[212:215], a[196:199], v[20:23]\n\t"
    "v_exp_f32 v163, v163\n\t"
    "v_mfma_f32_16x16x32_bf16 v[20:23], v[196:199], a[164:167], v[20:23]\n\t"
    "v_exp_f32 v168, v168\n\t"
    "v_mfma_f32_16x16x32_bf16 v[36:39], v[196:199], a[168:171], v[36:39]\n\t"
    "v_exp_f32 v169, v169\n\t"
    "v_mfma_f32_16x16x32_bf16 v[36:39], v[212:215], a[200:203], v[36:39]\n\t"
    "v_exp_f32 v170, v170\n\t"
    "v_mfma_f32_16x16x32_bf16 v[52:55], v[212:215], a[204:207], v[52:55]\n\t"
    "v_exp_f32 v171, v171\n\t"
    "v_mfma_f32_16x16x32_bf16 v[52:55], v[196:199], a[172:175], v[52:55]\n\t"
    "v_exp_f32 v176, v176\n\t"
    "v_mfma_f32_16x16x32_bf16 v[68:71], v[196:199], a[176:179], v[68:71]\n\t"
    "v_exp_f32 v177, v177\n\t"
    "v_mfma_f32_16x16x32_bf16 v[68:71], v[212:215], a[208:211], v[68:71]\n\t"
    "v_exp_f32 v178, v178\n\t"
    "v_mfma_f32_16x16x32_bf16 v[84:87], v[212:215], a[212:215], v[84:87]\n\t"
    "v_exp_f32 v179, v179\n\t"
    "v_mfma_f32_16x16x32_bf16 v[84:87], v[196:199], a[180:183], v[84:87]\n\t"
    "v_exp_f32 v184, v184\n\t"
    "v_mfma_f32_16x16x32_bf16 v[100:103], v[196:199], a[184:187], v[100:103]\n\t"
    "v_exp_f32 v185, v185\n\t"
    "v_mfma_f32_16x16x32_bf16 v[100:103], v[212:215], a[216:219], v[100:103]\n\t"
    "v_exp_f32 v186, v186\n\t"
    "v_mfma_f32_16x16x32_bf16 v[116:119], v[212:215], a[220:223], v[116:119]\n\t"
    "v_exp_f32 v187, v187\n\t"
    "v_mfma_f32_16x16x32_bf16 v[116:119], v[196:199], a[188:191], v[116:119]\n\t"
    "v_add_f32 v250, v250, v156\n\t"
    "v_add_f32 v251, v251, v157\n\t"
    "v_mfma_f32_16x16x32_bf16 v[8:11], v[200:203], a[160:163], v[8:11]\n\t"
    "v_exp_f32 v164, v164\n\t"
    "v_mfma_f32_16x16x32_bf16 v[8:11], v[216:219], a[192:195], v[8:11]\n\t"
    "v_exp_f32 v165, v165\n\t"
    "v_mfma_f32_16x16x32_bf16 v[24:27], v[216:219], a[196:199], v[24:27]\n\t"
    "v_exp_f32 v166, v166\n\t"
    "v_mfma_f32_16x16x32_bf16 v[24:27], v[200:203], a[164:167], v[24:27]\n\t"
    "v_exp_f32 v167, v167\n\t"
    "v_mfma_f32_16x16x32_bf16 v[40:43], v[200:203], a[168:171], v[40:43]\n\t"
    "v_exp_f32 v172, v172\n\t"
    "v_mfma_f32_16x16x32_bf16 v[40:43], v[216:219], a[200:203], v[40:43]\n\t"
    "v_exp_f32 v173, v173\n\t"
    "v_mfma_f32_16x16x32_bf16 v[56:59], v[216:219], a[204:207], v[56:59]\n\t"
    "v_exp_f32 v174, v174\n\t"
    "v_mfma_f32_16x16x32_bf16 v[56:59], v[200:203], a[172:175], v[56:59]\n\t"
    "v_exp_f32 v175, v175\n\t"
    "v_mfma_f32_16x16x32_bf16 v[72:75], v[200:203], a[176:179], v[72:75]\n\t"
    "v_exp_f32 v180, v180\n\t"
    "v_mfma_f32_16x16x32_bf16 v[72:75], v[216:219], a[208:211], v[72:75]\n\t"
    "v_exp_f32 v181, v181\n\t"
    "v_mfma_f32_16x16x32_bf16 v[88:91], v[216:219], a[212:215], v[88:91]\n\t"
    "v_exp_f32 v182, v182\n\t"
    "v_mfma_f32_16x16x32_bf16 v[88:91], v[200:203], a[180:183], v[88:91]\n\t"
    "v_exp_f32 v183, v183\n\t"
    "v_mfma_f32_16x16x32_bf16 v[104:107], v[200:203], a[184:187], v[104:107]\n\t"
    "s_add_u32 s56, s56, 65536\n\t"
    "v_exp_f32 v188, v188\n\t"
    "v_mfma_f32_16x16x32_bf16 v[104:107], v[216:219], a[216:219], v[104:107]\n\t"
    "s_xor_b32 s57, s57, 0x10000\n\t"
    "v_exp_f32 v189, v189\n\t"
    "v_mfma_f32_16x16x32_bf16 v[120:123], v[216:219], a[220:223], v[120:123]\n\t"
    "s_add_u32 s59, s59, 1\n\t"
    "v_exp_f32 v190, v190\n\t"
    "v_mfma_f32_16x16x32_bf16 v[120:123], v[200:203], a[188:191], v[120:123]\n\t"
    "s_and_b32 s60, s59, 7\n\t"
    "v_exp_f32 v191, v191\n\t"
    "v_mfma_f32_16x16x32_bf16 v[12:15], v[204:207], a[160:163], v[12:15]\n\t"
    "ds_read_b128 a[160:163], v240 offset:40960\n\t"
    "v_add_f32 v248, v248, v144\n\t"
    "v_mfma_f32_16x16x32_bf16 v[12:15], v[220:223], a[192:195], v[12:15]\n\t"
    "ds_read_b128 a[192:195], v240 offset:57344\n\t"
    "v_add_f32 v249, v249, v145\n\t"
    "v_mfma_f32_16x16x32_bf16 v[28:31], v[220:223], a[196:199], v[28:31]\n\t"
    "ds_read_b128 a[196:199], v240 offset:58368\n\t"
    "v_add_f32 v248, v248, v146\n\t"
    "v_mfma_f32_16x16x32_bf16 v[28:31], v[204:207], a[164:167], v[28:31]\n\t"
    "ds_read_b128 a[164:167], v240 offset:41984\n\t"
    "v_add_f32 v249, v249, v147\n\t"
    "v_mfma_f32_16x16x32_bf16 v[44:47], v[204:207], a[168:171], v[44:47]\n\t"
    "ds_read_b128 a[168:171], v240 offset:43008\n\t"
    "v_add_f32 v250, v250, v152\n\t"
    "v_mfma_f32_16x16x32_bf16 v[44:47], v[220:223], a[200:203], v[44:47]\n\t"
    "ds_read_b128 a[200:203], v240 offset:59392\n\t"
    "v_add_f32 v251, v251, v153\n\t"
    "v_mfma_f32_16x16x32_bf16 v[60:63], v[220:223], a[204:207], v[60:63]\n\t"
    "ds_read_b128 a[204:207], v240 offset:60416\n\t"
    "v_add_f32 v250, v250, v154\n\t"
    "v_mfma_f32_16x16x32_bf16 v[60:63], v[204:207], a[172:175], v[60:63]\n\t"
    "ds_read_b128 a[172:175], v240 offset:44032\n\t"
    "v_add_f32 v251, v251, v155\n\t"
    "v_mfma_f32_16x16x32_bf16 v[76:79], v[204:207], a[176:179], v[76:79]\n\t"
    "ds_read_b128 a[176:179], v240 offset:45056\n\t"
    "v_add_f32 v246, v246, v140\n\t"
    "v_mfma_f32_16x16x32_bf16 v[76:79], v[220:223], a[208:211], v[76:79]\n\t"
    "ds_read_b128 a[208:211], v240 offset:61440\n\t"
    "v_add_f32 v247, v247, v141\n\t"
    "v_mfma_f32_16x16x32_bf16 v[92:95], v[220:223], a[212:215], v[92:95]\n\t"
    "ds_read_b128 a[212:215], v240 offset:62464\n\t"
    "v_add_f32 v246, v246, v142\n\t"
    "v_mfma_f32_16x16x32_bf16 v[92:95], v[204:207], a[180:183], v[92:95]\n\t"
    "ds_read_b128 a[180:183], v240 offset:46080\n\t"
    "v_add_f32 v247, v247, v143\n\t"
    "v_mfma_f32_16x16x32_bf16 v[108:111], v[204:207], a[184:187], v[108:111]\n\t"
    "ds_read_b128 a[184:187], v240 offset:47104\n\t"
    "v_add_f32 v248, v248, v148\n\t"
    "v_mfma_f32_16x16x32_bf16 v[108:111], v[220:223], a[216:219], v[108:111]\n\t"
    "ds_read_b128 a[216:219], v240 offset:63488\n\t"
    "v_add_f32 v249, v249, v149\n\t"
    "v_mfma_f32_16x16x32_bf16 v[124:127], v[220:223], a[220:223], v[124:127]\n\t"
    "ds_read_b128 a[220:223], v240 offset:64512\n\t"
    "v_cvt_pk_bf16_f32 v192, v128, v129\n\t"
    "v_mfma_f32_16x16x32_bf16 v[124:127], v[204:207], a[188:191], v[124:127]\n\t"
    "s_waitcnt lgkmcnt(11)\n\t"
    "ds_read_b128 a[188:191], v240 offset:48128\n\t"
    "v_cvt_pk_bf16_f32 v193, v130, v131\n\t"
    "s_cbranch_scc1 L_top_%=\n\t"
    "s_cmp_eq_u32 s59, 16\n\t"
    "s_cbranch_scc1 L_done_%=\n\t"
    "v_accvgpr_mov_b32 a0, a64\n\t"
    "v_accvgpr_mov_b32 a1, a65\n\t"
    "v_accvgpr_mov_b32 a2, a66\n\t"
    "v_accvgpr_mov_b32 a3, a67\n\t"
    "v_accvgpr_mov_b32 a4, a68\n\t"
    "v_accvgpr_mov_b32 a5, a69\n\t"
    "v_accvgpr_mov_b32 a6, a70\n\t"
    "v_accvgpr_mov_b32 a7, a71\n\t"
    "v_accvgpr_mov_b32 a8, a72\n\t"
    "v_accvgpr_mov_b32 a9, a73\n\t"
    "v_accvgpr_mov_b32 a10, a74\n\t"
    "v_accvgpr_mov_b32 a11, a75\n\t"
    "v_accvgpr_mov_b32 a12, a76\n\t"
    "v_accvgpr_mov_b32 a13, a77\n\t"
    "v_accvgpr_mov_b32 a14, a78\n\t"
    "v_accvgpr_mov_b32 a15, a79\n\t"
    "v_accvgpr_mov_b32 a16, a80\n\t"
    "v_accvgpr_mov_b32 a17, a81\n\t"
    "v_accvgpr_mov_b32 a18, a82\n\t"
    "v_accvgpr_mov_b32 a19, a83\n\t"
    "v_accvgpr_mov_b32 a20, a84\n\t"
    "v_accvgpr_mov_b32 a21, a85\n\t"
    "v_accvgpr_mov_b32 a22, a86\n\t"
    "v_accvgpr_mov_b32 a23, a87\n\t"
    "v_accvgpr_mov_b32 a24, a88\n\t"
    "v_accvgpr_mov_b32 a25, a89\n\t"
    "v_accvgpr_mov_b32 a26, a90\n\t"
    "v_accvgpr_mov_b32 a27, a91\n\t"
    "v_accvgpr_mov_b32 a28, a92\n\t"
    "v_accvgpr_mov_b32 a29, a93\n\t"
    "v_accvgpr_mov_b32 a30, a94\n\t"
    "v_accvgpr_mov_b32 a31, a95\n\t"
    "v_accvgpr_mov_b32 a32, a96\n\t"
    "v_accvgpr_mov_b32 a33, a97\n\t"
    "v_accvgpr_mov_b32 a34, a98\n\t"
    "v_accvgpr_mov_b32 a35, a99\n\t"
    "v_accvgpr_mov_b32 a36, a100\n\t"
    "v_accvgpr_mov_b32 a37, a101\n\t"
    "v_accvgpr_mov_b32 a38, a102\n\t"
    "v_accvgpr_mov_b32 a39, a103\n\t"
    "v_accvgpr_mov_b32 a40, a104\n\t"
    "v_accvgpr_mov_b32 a41, a105\n\t"
    "v_accvgpr_mov_b32 a42, a106\n\t"
    "v_accvgpr_mov_b32 a43, a107\n\t"
    "v_accvgpr_mov_b32 a44, a108\n\t"
    "v_accvgpr_mov_b32 a45, a109\n\t"
    "v_accvgpr_mov_b32 a46, a110\n\t"
    "v_accvgpr_mov_b32 a47, a111\n\t"
    "v_accvgpr_mov_b32 a48, a112\n\t"
    "v_accvgpr_mov_b32 a49, a113\n\t"
    "v_accvgpr_mov_b32 a50, a114\n\t"
    "v_accvgpr_mov_b32 a51, a115\n\t"
    "v_accvgpr_mov_b32 a52, a116\n\t"
    "v_accvgpr_mov_b32 a53, a117\n\t"
    "v_accvgpr_mov_b32 a54, a118\n\t"
    "v_accvgpr_mov_b32 a55, a119\n\t"
    "v_accvgpr_mov_b32 a56, a120\n\t"
    "v_accvgpr_mov_b32 a57, a121\n\t"
    "v_accvgpr_mov_b32 a58, a122\n\t"
    "v_accvgpr_mov_b32 a59, a123\n\t"
    "v_accvgpr_mov_b32 a60, a124\n\t"
    "v_accvgpr_mov_b32 a61, a125\n\t"
    "v_accvgpr_mov_b32 a62, a126\n\t"
    "v_accvgpr_mov_b32 a63, a127\n\t"
    "v_mov_b32 v243, 0x71800000\n\t"
    "v_accvgpr_read_b32 v242, a244\n\t"
    "v_ldexp_f32 v242, v242, 1\n\t"
    "v_cmp_nlt_f32 vcc, |v242|, v243\n\t"
    "s_or_b64 s[62:63], s[62:63], vcc\n\t"
    "v_add_f32 v224, v224, v242\n\t"
    "v_add_f32 v225, v225, v242\n\t"
    "v_add_f32 v226, v226, v242\n\t"
    "v_add_f32 v227, v227, v242\n\t"
    "v_accvgpr_read_b32 v242, a245\n\t"
    "v_ldexp_f32 v242, v242, 1\n\t"
    "v_cmp_nlt_f32 vcc, |v242|, v243\n\t"
    "s_or_b64 s[62:63], s[62:63], vcc\n\t"
    "v_add_f32 v228, v228, v242\n\t"
    "v_add_f32 v229, v229, v242\n\t"
    "v_add_f32 v230, v230, v242\n\t"
    "v_add_f32 v231, v231, v242\n\t"
    "v_accvgpr_read_b32 v242, a246\n\t"
    "v_ldexp_f32 v242, v242, 1\n\t"
    "v_cmp_nlt_f32 vcc, |v242|, v243\n\t"
    "s_or_b64 s[62:63], s[62:63], vcc\n\t"
    "v_add_f32 v232, v232, v242\n\t"
    "v_add_f32 v233, v233, v242\n\t"
    "v_add_f32 v234, v234, v242\n\t"
    "v_add_f32 v235, v235, v242\n\t"
    "v_accvgpr_read_b32 v242, a247\n\t"
    "v_ldexp_f32 v242, v242, 1\n\t"
    "v_cmp_nlt_f32 vcc, |v242|, v243\n\t"
    "s_or_b64 s[62:63], s[62:63], vcc\n\t"
    "v_add_f32 v236, v236, v242\n\t"
    "v_add_f32 v237, v237, v242\n\t"
    "v_add_f32 v238, v238, v242\n\t"
    "v_add_f32 v239, v239, v242\n\t"
    "s_nop 1\n\t"
    "s_branch L_top_%=\n\t"
    "L_done_%=:\n\t"
    "v_cvt_pk_bf16_f32 v194, v132, v133\n\t"
    "v_cvt_pk_bf16_f32 v195, v134, v135\n\t"
    "v_cvt_pk_bf16_f32 v208, v160, v161\n\t"
    "v_cvt_pk_bf16_f32 v209, v162, v163\n\t"
    "v_cvt_pk_bf16_f32 v210, v164, v165\n\t"
    "v_cvt_pk_bf16_f32 v211, v166, v167\n\t"
    "s_waitcnt lgkmcnt(8)\n\t"
    "v_mfma_f32_16x16x32_bf16 a[228:231], v[192:195], a[224:227], a[228:231]\n\t"
    "v_cvt_pk_bf16_f32 v196, v136, v137\n\t"
    "v_cvt_pk_bf16_f32 v197, v138, v139\n\t"
    "v_mfma_f32_16x16x32_bf16 a[228:231], v[208:211], a[224:227], a[228:231]\n\t"
    "v_cvt_pk_bf16_f32 v198, v140, v141\n\t"
    "v_cvt_pk_bf16_f32 v199, v142, v143\n\t"
    "v_mfma_f32_16x16x32_bf16 v[0:3], v[208:211], a[192:195], v[0:3]\n\t"
    "v_cvt_pk_bf16_f32 v212, v168, v169\n\t"
    "v_cvt_pk_bf16_f32 v213, v170, v171\n\t"
    "v_mfma_f32_16x16x32_bf16 v[0:3], v[192:195], a[160:163], v[0:3]\n\t"
    "v_cvt_pk_bf16_f32 v214, v172, v173\n\t"
    "v_cvt_pk_bf16_f32 v215, v174, v175\n\t"
    "v_mfma_f32_16x16x32_bf16 v[16:19], v[192:195], a[164:167], v[16:19]\n\t"
    "v_and_b32 v242, 0xf0, v241\n\t"
    "v_lshrrev_b32 v243, 8, v241\n\t"
    "v_mfma_f32_16x16x32_bf16 v[16:19], v[208:211], a[196:199], v[16:19]\n\t"
    "v_lshl_add_u32 v242, v243, 10, v242\n\t"
    "v_mov_b32 v243, 1.0\n\t"
    "v_mfma_f32_16x16x32_bf16 v[32:35], v[208:211], a[200:203], v[32:35]\n\t"
    "v_accvgpr_read_b32 v252, a228\n\t"
    "v_accvgpr_read_b32 v253, a229\n\t"
    "v_mfma_f32_16x16x32_bf16 v[32:35], v[192:195], a[168:171], v[32:35]\n\t"
    "v_accvgpr_read_b32 v254, a230\n\t"
    "v_accvgpr_read_b32 v255, a231\n\t"
    "v_mfma_f32_16x16x32_bf16 v[48:51], v[192:195], a[172:175], v[48:51]\n\t"
    "s_waitcnt lgkmcnt(0)\n\t"
    "v_rcp_f32_dpp v224, v252 row_newbcast:0 row_mask:0xf bank_mask:0xf\n\t"
    "v_mfma_f32_16x16x32_bf16 v[48:51], v[208:211], a[204:207], v[48:51]\n\t"
    "v_rcp_f32_dpp v225, v253 row_newbcast:0 row_mask:0xf bank_mask:0xf\n\t"
    "v_mfma_f32_16x16x32_bf16 v[64:67], v[208:211], a[208:211], v[64:67]\n\t"
    "v_rcp_f32_dpp v226, v254 row_newbcast:0 row_mask:0xf bank_mask:0xf\n\t"
    "v_mfma_f32_16x16x32_bf16 v[64:67], v[192:195], a[176:179], v[64:67]\n\t"
    "v_rcp_f32_dpp v227, v255 row_newbcast:0 row_mask:0xf bank_mask:0xf\n\t"
    "v_mfma_f32_16x16x32_bf16 v[80:83], v[192:195], a[180:183], v[80:83]\n\t"
    "v_cvt_pk_bf16_f32 v200, v144, v145\n\t"
    "v_cvt_pk_bf16_f32 v201, v146, v147\n\t"
    "v_mfma_f32_16x16x32_bf16 v[80:83], v[208:211], a[212:215], v[80:83]\n\t"
    "v_cvt_pk_bf16_f32 v202, v148, v149\n\t"
    "v_cvt_pk_bf16_f32 v203, v150, v151\n\t"
    "v_mfma_f32_16x16x32_bf16 v[96:99], v[208:211], a[216:219], v[96:99]\n\t"
    "v_cvt_pk_bf16_f32 v216, v176, v177\n\t"
    "v_cvt_pk_bf16_f32 v217, v178, v179\n\t"
    "v_mfma_f32_16x16x32_bf16 v[96:99], v[192:195], a[184:187], v[96:99]\n\t"
    "v_cvt_pk_bf16_f32 v218, v180, v181\n\t"
    "v_cvt_pk_bf16_f32 v219, v182, v183\n\t"
    "v_mfma_f32_16x16x32_bf16 v[112:115], v[192:195], a[188:191], v[112:115]\n\t"
    "v_mfma_f32_16x16x32_bf16 v[112:115], v[208:211], a[220:223], v[112:115]\n\t"
    "v_mfma_f32_16x16x32_bf16 v[4:7], v[212:215], a[192:195], v[4:7]\n\t"
    "v_mfma_f32_16x16x32_bf16 v[4:7], v[196:199], a[160:163], v[4:7]\n\t"
    "v_mfma_f32_16x16x32_bf16 v[20:23], v[196:199], a[164:167], v[20:23]\n\t"
    "s_mov_b32 s60, 0\n\t"
    "v_mul_f32 v0, v0, v224\n\t"
    "v_mul_f32 v1, v1, v225\n\t"
    "v_mfma_f32_16x16x32_bf16 v[20:23], v[212:215], a[196:199], v[20:23]\n\t"
    "v_mul_f32 v16, v16, v224\n\t"
    "v_mul_f32 v17, v17, v225\n\t"
    "v_mfma_f32_16x16x32_bf16 v[36:39], v[212:215], a[200:203], v[36:39]\n\t"
    "v_mul_f32 v32, v32, v224\n\t"
    "v_mul_f32 v33, v33, v225\n\t"
    "v_mfma_f32_16x16x32_bf16 v[36:39], v[196:199], a[168:171], v[36:39]\n\t"
    "v_mul_f32 v48, v48, v224\n\t"
    "v_mul_f32 v49, v49, v225\n\t"
    "v_mfma_f32_16x16x32_bf16 v[52:55], v[196:199], a[172:175], v[52:55]\n\t"
    "v_mul_f32 v64, v64, v224\n\t"
    "v_mul_f32 v65, v65, v225\n\t"
    "v_mfma_f32_16x16x32_bf16 v[52:55], v[212:215], a[204:207], v[52:55]\n\t"
    "v_mul_f32 v80, v80, v224\n\t"
    "v_mul_f32 v81, v81, v225\n\t"
    "v_mfma_f32_16x16x32_bf16 v[68:71], v[212:215], a[208:211], v[68:71]\n\t"
    "v_mul_f32 v96, v96, v224\n\t"
    "v_mul_f32 v97, v97, v225\n\t"
    "v_mfma_f32_16x16x32_bf16 v[68:71], v[196:199], a[176:179], v[68:71]\n\t"
    "v_mul_f32 v112, v112, v224\n\t"
    "v_mul_f32 v113, v113, v225\n\t"
    "v_mfma_f32_16x16x32_bf16 v[84:87], v[196:199], a[180:183], v[84:87]\n\t"
    "v_cvt_pk_bf16_f32 v128, v0, v16\n\t"
    "v_cvt_pk_bf16_f32 v129, v32, v48\n\t"
    "v_mfma_f32_16x16x32_bf16 v[84:87], v[212:215], a[212:215], v[84:87]\n\t"
    "v_cvt_pk_bf16_f32 v130, v64, v80\n\t"
    "v_cvt_pk_bf16_f32 v131, v96, v112\n\t"
    "v_mfma_f32_16x16x32_bf16 v[100:103], v[212:215], a[216:219], v[100:103]\n\t"
    "buffer_store_dwordx4 v[128:131], v242, s[48:51], s60 offen offset:0 sc1\n\t"
    "v_mfma_f32_16x16x32_bf16 v[100:103], v[196:199], a[184:187], v[100:103]\n\t"
    "v_cvt_pk_bf16_f32 v132, v1, v17\n\t"
    "v_cvt_pk_bf16_f32 v133, v33, v49\n\t"
    "v_mfma_f32_16x16x32_bf16 v[116:119], v[196:199], a[188:191], v[116:119]\n\t"
    "v_cvt_pk_bf16_f32 v134, v65, v81\n\t"
    "v_cvt_pk_bf16_f32 v135, v97, v113\n\t"
    "v_mfma_f32_16x16x32_bf16 v[116:119], v[212:215], a[220:223], v[116:119]\n\t"
    "buffer_store_dwordx4 v[132:135], v242, s[48:51], s60 offen offset:256 sc1\n\t"
    "v_mfma_f32_16x16x32_bf16 v[8:11], v[216:219], a[192:195], v[8:11]\n\t"
    "v_add_f32 v246, v246, v168\n\t"
    "v_add_f32 v247, v247, v169\n\t"
    "v_mfma_f32_16x16x32_bf16 v[8:11], v[200:203], a[160:163], v[8:11]\n\t"
    "v_add_f32 v246, v246, v170\n\t"
    "v_add_f32 v247, v247, v171\n\t"
    "v_mfma_f32_16x16x32_bf16 v[24:27], v[200:203], a[164:167], v[24:27]\n\t"
    "v_add_f32 v246, v246, v172\n\t"
    "v_add_f32 v247, v247, v173\n\t"
    "v_mfma_f32_16x16x32_bf16 v[24:27], v[216:219], a[196:199], v[24:27]\n\t"
    "v_add_f32 v246, v246, v174\n\t"
    "v_add_f32 v247, v247, v175\n\t"
    "v_mfma_f32_16x16x32_bf16 v[40:43], v[216:219], a[200:203], v[40:43]\n\t"
    "v_add_f32 v246, v246, v247\n\t"
    "s_nop 1\n\t"
    "v_mfma_f32_16x16x32_bf16 v[40:43], v[200:203], a[168:171], v[40:43]\n\t"
    "v_mfma_f32_16x16x4_f32 v[228:231], v246, v243, 0\n\t"
    "v_mfma_f32_16x16x32_bf16 v[56:59], v[200:203], a[172:175], v[56:59]\n\t"
    "v_cvt_pk_bf16_f32 v204, v152, v153\n\t"
    "v_cvt_pk_bf16_f32 v205, v154, v155\n\t"
    "v_mfma_f32_16x16x32_bf16 v[56:59], v[216:219], a[204:207], v[56:59]\n\t"
    "v_cvt_pk_bf16_f32 v206, v156, v157\n\t"
    "v_cvt_pk_bf16_f32 v207, v158, v159\n\t"
    "v_mfma_f32_16x16x32_bf16 v[72:75], v[216:219], a[208:211], v[72:75]\n\t"
    "v_cvt_pk_bf16_f32 v220, v184, v185\n\t"
    "v_cvt_pk_bf16_f32 v221, v186, v187\n\t"
    "v_mfma_f32_16x16x32_bf16 v[72:75], v[200:203], a[176:179], v[72:75]\n\t"
    "v_cvt_pk_bf16_f32 v222, v188, v189\n\t"
    "v_cvt_pk_bf16_f32 v223, v190, v191\n\t"
    "v_mfma_f32_16x16x32_bf16 v[88:91], v[200:203], a[180:183], v[88:91]\n\t"
    "v_add_f32 v248, v248, v150\n\t"
    "v_add_f32 v249, v249, v151\n\t"
    "v_mfma_f32_16x16x32_bf16 v[88:91], v[216:219], a[212:215], v[88:91]\n\t"
    "v_add_f32 v248, v248, v176\n\t"
    "v_add_f32 v249, v249, v177\n\t"
    "v_mfma_f32_16x16x32_bf16 v[104:107], v[216:219], a[216:219], v[104:107]\n\t"
    "v_add_f32 v248, v248, v178\n\t"
    "v_add_f32 v249, v249, v179\n\t"
    "v_mfma_f32_16x16x32_bf16 v[104:107], v[200:203], a[184:187], v[104:107]\n\t"
    "v_add_f32 v248, v248, v180\n\t"
    "v_add_f32 v249, v249, v181\n\t"
    "v_mfma_f32_16x16x32_bf16 v[120:123], v[200:203], a[188:191], v[120:123]\n\t"
    "v_add_f32 v248, v248, v182\n\t"
    "v_add_f32 v249, v249, v183\n\t"
    "v_mfma_f32_16x16x32_bf16 v[120:123], v[216:219], a[220:223], v[120:123]\n\t"
    "v_add_f32 v248, v248, v249\n\t"
    "s_nop 1\n\t"
    "v_mfma_f32_16x16x32_bf16 v[12:15], v[220:223], a[192:195], v[12:15]\n\t"
    "v_mfma_f32_16x16x4_f32 v[232:235], v248, v243, 0\n\t"
    "v_mfma_f32_16x16x32_bf16 v[12:15], v[204:207], a[160:163], v[12:15]\n\t"
    "v_rcp_f32 v228, v228\n\t"
    "v_mfma_f32_16x16x32_bf16 v[28:31], v[204:207], a[164:167], v[28:31]\n\t"
    "v_rcp_f32 v229, v229\n\t"
    "v_mfma_f32_16x16x32_bf16 v[28:31], v[220:223], a[196:199], v[28:31]\n\t"
    "v_rcp_f32 v230, v230\n\t"
    "v_mfma_f32_16x16x32_bf16 v[44:47], v[220:223], a[200:203], v[44:47]\n\t"
    "v_rcp_f32 v231, v231\n\t"
    "v_mfma_f32_16x16x32_bf16 v[44:47], v[204:207], a[168:171], v[44:47]\n\t"
    "v_add_f32 v250, v250, v158\n\t"
    "v_add_f32 v251, v251, v159\n\t"
    "v_mfma_f32_16x16x32_bf16 v[60:63], v[204:207], a[172:175], v[60:63]\n\t"
    "v_add_f32 v250, v250, v184\n\t"
    "v_add_f32 v251, v251, v185\n\t"
    "v_mfma_f32_16x16x32_bf16 v[60:63], v[220:223], a[204:207], v[60:63]\n\t"
    "v_add_f32 v250, v250, v186\n\t"
    "v_add_f32 v251, v251, v187\n\t"
    "v_mfma_f32_16x16x32_bf16 v[76:79], v[220:223], a[208:211], v[76:79]\n\t"
    "v_add_f32 v250, v250, v188\n\t"
    "v_add_f32 v251, v251, v189\n\t"
    "v_mfma_f32_16x16x32_bf16 v[76:79], v[204:207], a[176:179], v[76:79]\n\t"
    "v_add_f32 v250, v250, v190\n\t"
    "v_add_f32 v251, v251, v191\n\t"
    "v_mfma_f32_16x16x32_bf16 v[92:95], v[204:207], a[180:183], v[92:95]\n\t"
    "v_add_f32 v250, v250, v251\n\t"
    "s_nop 1\n\t"
    "v_mfma_f32_16x16x32_bf16 v[92:95], v[220:223], a[212:215], v[92:95]\n\t"
    "v_mfma_f32_16x16x4_f32 v[236:239], v250, v243, 0\n\t"
    "v_mfma_f32_16x16x32_bf16 v[108:111], v[220:223], a[216:219], v[108:111]\n\t"
    "v_rcp_f32 v232, v232\n\t"
    "v_mfma_f32_16x16x32_bf16 v[108:111], v[204:207], a[184:187], v[108:111]\n\t"
    "v_rcp_f32 v233, v233\n\t"
    "v_mfma_f32_16x16x32_bf16 v[124:127], v[204:207], a[188:191], v[124:127]\n\t"
    "v_rcp_f32 v234, v234\n\t"
    "v_mfma_f32_16x16x32_bf16 v[124:127], v[220:223], a[220:223], v[124:127]\n\t"
    "v_rcp_f32 v235, v235\n\t"
    "s_nop 3\n\t"
    "v_rcp_f32 v236, v236\n\t"
    "v_rcp_f32 v237, v237\n\t"
    "v_rcp_f32 v238, v238\n\t"
    "v_rcp_f32 v239, v239\n\t"
    "s_mov_b32 s60, 0\n\t"
    "v_pk_mul_f32 v[2:3], v[2:3], v[226:227]\n\t"
    "v_pk_mul_f32 v[18:19], v[18:19], v[226:227]\n\t"
    "v_pk_mul_f32 v[34:35], v[34:35], v[226:227]\n\t"
    "v_pk_mul_f32 v[50:51], v[50:51], v[226:227]\n\t"
    "v_pk_mul_f32 v[66:67], v[66:67], v[226:227]\n\t"
    "v_pk_mul_f32 v[82:83], v[82:83], v[226:227]\n\t"
    "v_pk_mul_f32 v[98:99], v[98:99], v[226:227]\n\t"
    "v_pk_mul_f32 v[114:115], v[114:115], v[226:227]\n\t"
    "v_cvt_pk_bf16_f32 v160, v2, v18\n\t"
    "v_cvt_pk_bf16_f32 v161, v34, v50\n\t"
    "v_cvt_pk_bf16_f32 v162, v66, v82\n\t"
    "v_cvt_pk_bf16_f32 v163, v98, v114\n\t"
    "buffer_store_dwordx4 v[160:163], v242, s[48:51], s60 offen offset:512 sc1\n\t"
    "v_cvt_pk_bf16_f32 v164, v3, v19\n\t"
    "v_cvt_pk_bf16_f32 v165, v35, v51\n\t"
    "v_cvt_pk_bf16_f32 v166, v67, v83\n\t"
    "v_cvt_pk_bf16_f32 v167, v99, v115\n\t"
    "buffer_store_dwordx4 v[164:167], v242, s[48:51], s60 offen offset:768 sc1\n\t"
    "s_mov_b32 s60, 4096\n\t"
    "v_pk_mul_f32 v[4:5], v[4:5], v[228:229]\n\t"
    "v_pk_mul_f32 v[20:21], v[20:21], v[228:229]\n\t"
    "v_pk_mul_f32 v[36:37], v[36:37], v[228:229]\n\t"
    "v_pk_mul_f32 v[52:53], v[52:53], v[228:229]\n\t"
    "v_pk_mul_f32 v[68:69], v[68:69], v[228:229]\n\t"
    "v_pk_mul_f32 v[84:85], v[84:85], v[228:229]\n\t"
    "v_pk_mul_f32 v[100:101], v[100:101], v[228:229]\n\t"
    "v_pk_mul_f32 v[116:117], v[116:117], v[228:229]\n\t"
    "v_cvt_pk_bf16_f32 v136, v4, v20\n\t"
    "v_cvt_pk_bf16_f32 v137, v36, v52\n\t"
    "v_cvt_pk_bf16_f32 v138, v68, v84\n\t"
    "v_cvt_pk_bf16_f32 v139, v100, v116\n\t"
    "buffer_store_dwordx4 v[136:139], v242, s[48:51], s60 offen offset:0 sc1\n\t"
    "v_cvt_pk_bf16_f32 v140, v5, v21\n\t"
    "v_cvt_pk_bf16_f32 v141, v37, v53\n\t"
    "v_cvt_pk_bf16_f32 v142, v69, v85\n\t"
    "v_cvt_pk_bf16_f32 v143, v101, v117\n\t"
    "buffer_store_dwordx4 v[140:143], v242, s[48:51], s60 offen offset:256 sc1\n\t"
    "s_mov_b32 s60, 4096\n\t"
    "v_pk_mul_f32 v[6:7], v[6:7], v[230:231]\n\t"
    "v_pk_mul_f32 v[22:23], v[22:23], v[230:231]\n\t"
    "v_pk_mul_f32 v[38:39], v[38:39], v[230:231]\n\t"
    "v_pk_mul_f32 v[54:55], v[54:55], v[230:231]\n\t"
    "v_pk_mul_f32 v[70:71], v[70:71], v[230:231]\n\t"
    "v_pk_mul_f32 v[86:87], v[86:87], v[230:231]\n\t"
    "v_pk_mul_f32 v[102:103], v[102:103], v[230:231]\n\t"
    "v_pk_mul_f32 v[118:119], v[118:119], v[230:231]\n\t"
    "v_cvt_pk_bf16_f32 v168, v6, v22\n\t"
    "v_cvt_pk_bf16_f32 v169, v38, v54\n\t"
    "v_cvt_pk_bf16_f32 v170, v70, v86\n\t"
    "v_cvt_pk_bf16_f32 v171, v102, v118\n\t"
    "buffer_store_dwordx4 v[168:171], v242, s[48:51], s60 offen offset:512 sc1\n\t"
    "v_cvt_pk_bf16_f32 v172, v7, v23\n\t"
    "v_cvt_pk_bf16_f32 v173, v39, v55\n\t"
    "v_cvt_pk_bf16_f32 v174, v71, v87\n\t"
    "v_cvt_pk_bf16_f32 v175, v103, v119\n\t"
    "buffer_store_dwordx4 v[172:175], v242, s[48:51], s60 offen offset:768 sc1\n\t"
    "s_mov_b32 s60, 8192\n\t"
    "v_pk_mul_f32 v[8:9], v[8:9], v[232:233]\n\t"
    "v_pk_mul_f32 v[24:25], v[24:25], v[232:233]\n\t"
    "v_pk_mul_f32 v[40:41], v[40:41], v[232:233]\n\t"
    "v_pk_mul_f32 v[56:57], v[56:57], v[232:233]\n\t"
    "v_pk_mul_f32 v[72:73], v[72:73], v[232:233]\n\t"
    "v_pk_mul_f32 v[88:89], v[88:89], v[232:233]\n\t"
    "v_pk_mul_f32 v[104:105], v[104:105], v[232:233]\n\t"
    "v_pk_mul_f32 v[120:121], v[120:121], v[232:233]\n\t"
    "v_cvt_pk_bf16_f32 v144, v8, v24\n\t"
    "v_cvt_pk_bf16_f32 v145, v40, v56\n\t"
    "v_cvt_pk_bf16_f32 v146, v72, v88\n\t"
    "v_cvt_pk_bf16_f32 v147, v104, v120\n\t"
    "buffer_store_dwordx4 v[144:147], v242, s[48:51], s60 offen offset:0 sc1\n\t"
    "v_cvt_pk_bf16_f32 v148, v9, v25\n\t"
    "v_cvt_pk_bf16_f32 v149, v41, v57\n\t"
    "v_cvt_pk_bf16_f32 v150, v73, v89\n\t"
    "v_cvt_pk_bf16_f32 v151, v105, v121\n\t"
    "buffer_store_dwordx4 v[148:151], v242, s[48:51], s60 offen offset:256 sc1\n\t"
    "s_mov_b32 s60, 8192\n\t"
    "v_pk_mul_f32 v[10:11], v[10:11], v[234:235]\n\t"
    "v_pk_mul_f32 v[26:27], v[26:27], v[234:235]\n\t"
    "v_pk_mul_f32 v[42:43], v[42:43], v[234:235]\n\t"
    "v_pk_mul_f32 v[58:59], v[58:59], v[234:235]\n\t"
    "v_pk_mul_f32 v[74:75], v[74:75], v[234:235]\n\t"
    "v_pk_mul_f32 v[90:91], v[90:91], v[234:235]\n\t"
    "v_pk_mul_f32 v[106:107], v[106:107], v[234:235]\n\t"
    "v_pk_mul_f32 v[122:123], v[122:123], v[234:235]\n\t"
    "v_cvt_pk_bf16_f32 v176, v10, v26\n\t"
    "v_cvt_pk_bf16_f32 v177, v42, v58\n\t"
    "v_cvt_pk_bf16_f32 v178, v74, v90\n\t"
    "v_cvt_pk_bf16_f32 v179, v106, v122\n\t"
    "buffer_store_dwordx4 v[176:179], v242, s[48:51], s60 offen offset:512 sc1\n\t"
    "v_cvt_pk_bf16_f32 v180, v11, v27\n\t"
    "v_cvt_pk_bf16_f32 v181, v43, v59\n\t"
    "v_cvt_pk_bf16_f32 v182, v75, v91\n\t"
    "v_cvt_pk_bf16_f32 v183, v107, v123\n\t"
    "buffer_store_dwordx4 v[180:183], v242, s[48:51], s60 offen offset:768 sc1\n\t"
    "s_mov_b32 s60, 12288\n\t"
    "v_pk_mul_f32 v[12:13], v[12:13], v[236:237]\n\t"
    "v_pk_mul_f32 v[28:29], v[28:29], v[236:237]\n\t"
    "v_pk_mul_f32 v[44:45], v[44:45], v[236:237]\n\t"
    "v_pk_mul_f32 v[60:61], v[60:61], v[236:237]\n\t"
    "v_pk_mul_f32 v[76:77], v[76:77], v[236:237]\n\t"
    "v_pk_mul_f32 v[92:93], v[92:93], v[236:237]\n\t"
    "v_pk_mul_f32 v[108:109], v[108:109], v[236:237]\n\t"
    "v_pk_mul_f32 v[124:125], v[124:125], v[236:237]\n\t"
    "v_cvt_pk_bf16_f32 v152, v12, v28\n\t"
    "v_cvt_pk_bf16_f32 v153, v44, v60\n\t"
    "v_cvt_pk_bf16_f32 v154, v76, v92\n\t"
    "v_cvt_pk_bf16_f32 v155, v108, v124\n\t"
    "buffer_store_dwordx4 v[152:155], v242, s[48:51], s60 offen offset:0 sc1\n\t"
    "v_cvt_pk_bf16_f32 v156, v13, v29\n\t"
    "v_cvt_pk_bf16_f32 v157, v45, v61\n\t"
    "v_cvt_pk_bf16_f32 v158, v77, v93\n\t"
    "v_cvt_pk_bf16_f32 v159, v109, v125\n\t"
    "buffer_store_dwordx4 v[156:159], v242, s[48:51], s60 offen offset:256 sc1\n\t"
    "s_mov_b32 s60, 12288\n\t"
    "v_pk_mul_f32 v[14:15], v[14:15], v[238:239]\n\t"
    "v_pk_mul_f32 v[30:31], v[30:31], v[238:239]\n\t"
    "v_pk_mul_f32 v[46:47], v[46:47], v[238:239]\n\t"
    "v_pk_mul_f32 v[62:63], v[62:63], v[238:239]\n\t"
    "v_pk_mul_f32 v[78:79], v[78:79], v[238:239]\n\t"
    "v_pk_mul_f32 v[94:95], v[94:95], v[238:239]\n\t"
    "v_pk_mul_f32 v[110:111], v[110:111], v[238:239]\n\t"
    "v_pk_mul_f32 v[126:127], v[126:127], v[238:239]\n\t"
    "v_cvt_pk_bf16_f32 v184, v14, v30\n\t"
    "v_cvt_pk_bf16_f32 v185, v46, v62\n\t"
    "v_cvt_pk_bf16_f32 v186, v78, v94\n\t"
    "v_cvt_pk_bf16_f32 v187, v110, v126\n\t"
    "buffer_store_dwordx4 v[184:187], v242, s[48:51], s60 offen offset:512 sc1\n\t"
    "v_cvt_pk_bf16_f32 v188, v15, v31\n\t"
    "v_cvt_pk_bf16_f32 v189, v47, v63\n\t"
    "v_cvt_pk_bf16_f32 v190, v79, v95\n\t"
    "v_cvt_pk_bf16_f32 v191, v111, v127\n\t"
    "buffer_store_dwordx4 v[188:191], v242, s[48:51], s60 offen offset:768 sc1\n\t"
    "v_mov_b32 v253, 132624\n\t"
    "ds_read_b64 v[254:255], v253\n\t"
    "v_accvgpr_read_b32 v192, a228\n\t"
    "v_accvgpr_read_b32 v193, a229\n\t"
    "v_accvgpr_read_b32 v194, a230\n\t"
    "v_accvgpr_read_b32 v195, a231\n\t"
    "v_add_f32 v246, v246, v248\n\t"
    "v_add_f32 v246, v246, v250\n\t"
    "v_add_f32 v246, v246, v192\n\t"
    "v_add_f32 v246, v246, v193\n\t"
    "v_add_f32 v246, v246, v194\n\t"
    "v_add_f32 v246, v246, v195\n\t"
    "v_cmp_ngt_f32 vcc, 0x53800000, v246\n\t"
    "s_or_b64 s[62:63], s[62:63], vcc\n\t"
    "s_waitcnt lgkmcnt(0)\n\t"
    "v_or_b32 v254, v254, v255\n\t"
    "v_cmp_ne_u32 vcc, 0, v254\n\t"
    "s_or_b64 s[62:63], s[62:63], vcc\n\t"
    "s_or_b32 %0, s62, s63\n\t"
    "L_end_%=:\n\t"
    : "=&s"(bad)
    : "s"(__builtin_amdgcn_kernarg_segment_ptr()), "s"(b), "s"(wid)
    : "memory", "vcc", "scc", "s8", "s9", "s10", "s11", "s12", "s13", "s14", "s15", "s16", "s17", "s18", "s19", "s20", "s21", "s22", "s23", "s24", "s25", "s26", "s27", "s28", "s29", "s30", "s40", "s41", "s42", "s43", "s44", "s45", "s46", "s47", "s48", "s49", "s50", "s51", "s52", "s53", "s54", "s55", "s56", "s57", "s58", "s59", "s60", "s61", "s62", "s63", "s64", "s65", "s66", "s67", "v0", "v1", "v2", "v3", "v4", "v5", "v6", "v7", "v8", "v9", "v10", "v11", "v12", "v13", "v14", "v15", "v16", "v17", "v18", "v19", "v20", "v21", "v22", "v23", "v24", "v25", "v26", "v27", "v28", "v29", "v30", "v31", "v32", "v33", "v34", "v35", "v36", "v37", "v38", "v39", "v40", "v41", "v42", "v43", "v44", "v45", "v46", "v47", "v48", "v49", "v50", "v51", "v52", "v53", "v54", "v55", "v56", "v57", "v58", "v59", "v60", "v61", "v62", "v63", "v64", "v65", "v66", "v67", "v68", "v69", "v70", "v71", "v72", "v73", "v74", "v75", "v76", "v77", "v78", "v79", "v80", "v81", "v82", "v83", "v84", "v85", "v86", "v87", "v88", "v89", "v90", "v91", "v92", "v93", "v94", "v95", "v96", "v97", "v98", "v99", "v100", "v101", "v102", "v103", "v104", "v105", "v106", "v107", "v108", "v109", "v110", "v111", "v112", "v113", "v114", "v115", "v116", "v117", "v118", "v119", "v120", "v121", "v122", "v123", "v124", "v125", "v126", "v127", "v128", "v129", "v130", "v131", "v132", "v133", "v134", "v135", "v136", "v137", "v138", "v139", "v140", "v141", "v142", "v143", "v144", "v145", "v146", "v147", "v148", "v149", "v150", "v151", "v152", "v153", "v154", "v155", "v156", "v157", "v158", "v159", "v160", "v161", "v162", "v163", "v164", "v165", "v166", "v167", "v168", "v169", "v170", "v171", "v172", "v173", "v174", "v175", "v176", "v177", "v178", "v179", "v180", "v181", "v182", "v183", "v184", "v185", "v186", "v187", "v188", "v189", "v190", "v191", "v192", "v193", "v194", "v195", "v196", "v197", "v198", "v199", "v200", "v201", "v202", "v203", "v204", "v205", "v206", "v207", "v208", "v209", "v210", "v211", "v212", "v213", "v214", "v215", "v216", "v217", "v218", "v219", "v220", "v221", "v222", "v223", "v224", "v225", "v226", "v227", "v228", "v229", "v230", "v231", "v232", "v233", "v234", "v235", "v236", "v237", "v238", "v239", "v240", "v241", "v242", "v243", "v244", "v245", "v246", "v247", "v248", "v249", "v250", "v251", "v252", "v253", "v254", "v255", "a0", "a1", "a2", "a3", "a4", "a5", "a6", "a7", "a8", "a9", "a10", "a11", "a12", "a13", "a14", "a15", "a16", "a17", "a18", "a19", "a20", "a21", "a22", "a23", "a24", "a25", "a26", "a27", "a28", "a29", "a30", "a31", "a32", "a33", "a34", "a35", "a36", "a37", "a38", "a39", "a40", "a41", "a42", "a43", "a44", "a45", "a46", "a47", "a48", "a49", "a50", "a51", "a52", "a53", "a54", "a55", "a56", "a57", "a58", "a59", "a60", "a61", "a62", "a63", "a64", "a65", "a66", "a67", "a68", "a69", "a70", "a71", "a72", "a73", "a74", "a75", "a76", "a77", "a78", "a79", "a80", "a81", "a82", "a83", "a84", "a85", "a86", "a87", "a88", "a89", "a90", "a91", "a92", "a93", "a94", "a95", "a96", "a97", "a98", "a99", "a100", "a101", "a102", "a103", "a104", "a105", "a106", "a107", "a108", "a109", "a110", "a111", "a112", "a113", "a114", "a115", "a116", "a117", "a118", "a119", "a120", "a121", "a122", "a123", "a124", "a125", "a126", "a127", "a128", "a129", "a130", "a131", "a132", "a133", "a134", "a135", "a136", "a137", "a138", "a139", "a140", "a141", "a142", "a143", "a144", "a145", "a146", "a147", "a148", "a149", "a150", "a151", "a152", "a153", "a154", "a155", "a156", "a157", "a158", "a159", "a160", "a161", "a162", "a163", "a164", "a165", "a166", "a167", "a168", "a169", "a170", "a171", "a172", "a173", "a174", "a175", "a176", "a177", "a178", "a179", "a180", "a181", "a182", "a183", "a184", "a185", "a186", "a187", "a188", "a189", "a190", "a191", "a192", "a193", "a194", "a195", "a196", "a197", "a198", "a199", "a200", "a201", "a202", "a203", "a204", "a205", "a206", "a207", "a208", "a209", "a210", "a211", "a212", "a213", "a214", "a215", "a216", "a217", "a218", "a219", "a220", "a221", "a222", "a223", "a224", "a225", "a226", "a227", "a228", "a229", "a230", "a231", "a232", "a233", "a234", "a235", "a236", "a237", "a238", "a239", "a240", "a241", "a242", "a243", "a244", "a245", "a246", "a247");
  if (bad) {
    asm volatile("s_waitcnt vmcnt(0)" ::: "memory");
    const unsigned kvh = (b & 7u) >> 1, idx = (b >> 3) + 32u * (b & 1u);
    const size_t row0 = (size_t)(kvh * 64u + idx) * 256u + wid * 64u;
    const int lane = __builtin_amdgcn_mbcnt_hi(~0u, __builtin_amdgcn_mbcnt_lo(~0u, 0u));
    slow_rows(q + row0 * DH, qk + (size_t)kvh * SKV * DH, qv + (size_t)kvh * SKV * DH, ks + kvh * 2 * DH, kz + kvh * 2 * DH, vs + kvh * SKV, vz + kvh * SKV, out + row0 * DH, lane);
  }
}

extern "C" void kernel_launch(void* const* d_in, const int* in_sizes, int n_in, void* d_out, int out_size, void* d_ws, size_t ws_size, hipStream_t stream) {
  (void)in_sizes; (void)n_in; (void)out_size; (void)ws_size;
  (void)hipFuncSetAttribute((const void*)attn_kernel, hipFuncAttributeMaxDynamicSharedMemorySize, LDS_BYTES);
  hipLaunchKernelGGL(attn_kernel, dim3(256), dim3(256), LDS_BYTES, stream, (const u16*)d_in[0], (const int*)d_in[1], (const int*)d_in[2], (const u16*)d_in[3], (const u16*)d_in[4],
                     (const u16*)d_in[5], (const u16*)d_in[6], (char*)d_ws, (u16*)d_out);
}
```
